```python
import torch

def _softmax(x, axis=-1):
    return torch.softmax(x, dim=axis)
def _gelu(x):
    return torch.nn.functional.gelu(x, approximate="tanh")
def _silu(x):
    return torch.nn.functional.silu(x)
def _sigmoid(x):
    return torch.sigmoid(x)
def _layernorm(x, axis=-1, eps=1e-5):
    m = x.mean(dim=axis, keepdim=True)
    v = x.var(dim=axis, keepdim=True, unbiased=False)
    return (x - m) / torch.sqrt(v + eps)
def _scatter_add(tgt, idx, src, axis=0):
    return tgt.clone().index_add_(axis, idx, src)
def _segment_sum(src, seg, n):
    out = torch.zeros((n,) + src.shape[1:], dtype=src.dtype)
    return out.index_add_(0, seg, src)
def _pad(x, pw):
    flat = tuple(v for pair in reversed(pw) for v in pair)
    return torch.nn.functional.pad(x, flat)

kernel_name = 'gated_mix__spectral_gfnet__dense_activation_gelu__prompt'

def setup_inputs():

    _w = (lambda a: (lambda t: t.to(torch.bfloat16) if t.is_floating_point() else t)(torch.as_tensor(a)))
    def _A_setup():
        B, S, D = 1, 2048, 1024
        Df = D // 2 + 1
        return {
            "x": _w((lambda *s: torch.randn(*s, dtype=torch.bfloat16) * 0.05)(B, S, D)),
            "w_r": _w((lambda *s: torch.randn(*s, dtype=torch.bfloat16) * 3.2)(S, Df) * 0.02),
            "w_i": _w((lambda *s: torch.randn(*s, dtype=torch.bfloat16) * 3.2)(S, Df) * 0.02),
        }
    _A_in = _A_setup()

    def _B_setup():
        B, S, D, F = 1, 2048, 1024, 3584
        return {
            "x": _w((lambda *s: torch.randn(*s, dtype=torch.bfloat16) * 0.05)(B, S, D)),
            "w1": _w((lambda *s: torch.randn(*s, dtype=torch.bfloat16) * 3.2)(D, F) * (D ** -0.5)),
            "b1": _w((lambda *s: torch.randn(*s, dtype=torch.bfloat16) * 3.2)(F) * 0.01),
            "w2": _w((lambda *s: torch.randn(*s, dtype=torch.bfloat16) * 0.2)(F, D) * (F ** -0.5)),
            "b2": _w((lambda *s: torch.randn(*s, dtype=torch.bfloat16) * 0.4)(D) * 0.01),
        }
    _B_in = _B_setup()

    return {
        "A_x": _A_in["x"],
        "A_w_r": _A_in["w_r"],
        "A_w_i": _A_in["w_i"],
        "B_x": _B_in["x"],
        "B_w1": _B_in["w1"],
        "B_b1": _B_in["b1"],
        "B_w2": _B_in["w2"],
        "B_b2": _B_in["b2"],
        "gate_w": _w((lambda *s: torch.randn(*s, dtype=torch.bfloat16) * 0.05)(1024) * 16.0),
    }

def reference(A_x, A_w_r, A_w_i, B_x, B_w1, B_b1, B_w2, B_b2, gate_w):
    def _A_ref(x, w_r, w_i):
        B, S, D = x.shape
        X = (lambda x, axes=(-2,-1): torch.fft.rfft2(x.float(), dim=axes))(x, axes=(-2, -1))
        W = w_r + 1j * w_i
        X = X * W
        y = (lambda x, s, axes=(-2,-1): torch.fft.irfft2(x, s=s, dim=axes).to(torch.bfloat16))(X, (S, D), axes=(-2, -1))
        return y

    def _B_ref(x, w1, b1, w2, b2):
        h = torch.einsum("bsd,df->bsf", x, w1) + b1
        h = _gelu(h)
        return torch.einsum("bsf,fd->bsd", h, w2) + b2

    x = A_x
    g = _sigmoid((x * gate_w).sum(-1, keepdims=True))
    a = _A_ref(x, A_w_r, A_w_i)
    b = _B_ref(x, B_w1, B_b1, B_w2, B_b2)
    return g * a + (1 - g) * b
```

```cpp
#include <hip/hip_runtime.h>
#define TW_AFTER 1
#include <stdint.h>
#define LAS __attribute__((address_space(3)))
#define GAS __attribute__((address_space(1)))
typedef unsigned short u16;
typedef short s16x4 __attribute__((ext_vector_type(4)));
typedef short bf16x8 __attribute__((ext_vector_type(8)));
typedef float f32x2 __attribute__((ext_vector_type(2)));
typedef float f32x4 __attribute__((ext_vector_type(4)));
typedef float f32x16 __attribute__((ext_vector_type(16)));
typedef unsigned u32x2 __attribute__((ext_vector_type(2)));
typedef unsigned u32x4 __attribute__((ext_vector_type(4)));
typedef __bf16 bf16x2_t __attribute__((ext_vector_type(2)));
#define DI __device__ __forceinline__

DI unsigned cvtpk(float lo, float hi) { f32x2 v = {lo, hi}; bf16x2_t b = __builtin_convertvector(v, bf16x2_t); return __builtin_bit_cast(unsigned, b); }
DI float bf_lo(unsigned u) { return __uint_as_float(u << 16); }
DI float bf_hi(unsigned u) { return __uint_as_float(u & 0xffff0000u); }
DI float bf2f(u16 v) { return __uint_as_float(((unsigned)v) << 16); }
#define WAIT_V(n) asm volatile("s_waitcnt vmcnt(" #n ")" ::: "memory")
#define WAIT_L(n) asm volatile("s_waitcnt lgkmcnt(" #n ")" ::: "memory")
#define BAR __builtin_amdgcn_s_barrier()
#define SCHED0 __builtin_amdgcn_sched_barrier(0)
DI s16x4 tr_read(const char LAS* p) { return __builtin_amdgcn_ds_read_tr16_b64_v4i16((s16x4 LAS*)p); }
DI bf16x8 cat8(s16x4 a, s16x4 b) { return __builtin_shufflevector(a, b, 0, 1, 2, 3, 4, 5, 6, 7); }
DI void glds16(const void* g, char LAS* l) { __builtin_amdgcn_global_load_lds((const unsigned GAS*)g, (unsigned LAS*)l, 16, 0, 0); }
__device__ const unsigned __attribute__((aligned(256))) TAB_P1[2048] = {
0x3f803f80,0x3f803f80,0x3f803f80,0x3f803f80,0x80008000,0x80008000,0x80008000,0x80008000,0x3f7b3f80,0x3f553f6d,0x3f0e3f35,0x3e483ec4,
0xbe488000,0xbf0ebec4,0xbf55bf35,0xbf7bbf6d,0x3f6d3f80,0x3ec43f35,0xbec40000,0xbf6dbf35,0xbec48000,0xbf6dbf35,0xbf6dbf80,0xbec4bf35,
0x3f553f80,0xbe483ec4,0xbf7bbf35,0xbf0ebf6d,0xbf0e8000,0xbf7bbf6d,0xbe48bf35,0x3f553ec4,0x3f353f80,0xbf350000,0xbf35bf80,0x3f350000,
0xbf358000,0xbf35bf80,0x3f358000,0x3f353f80,0x3f0e3f80,0xbf7bbec4,0x3e48bf35,0x3f553f6d,0xbf558000,0xbe48bf6d,0x3f7b3f35,0xbf0e3ec4,
0x3ec43f80,0xbf6dbf35,0x3f6d0000,0xbec43f35,0xbf6d8000,0x3ec4bf35,0x3ec43f80,0xbf6dbf35,0x3e483f80,0xbf0ebf6d,0x3f553f35,0xbf7bbec4,
0xbf7b8000,0x3f55bec4,0xbf0e3f35,0x3e48bf6d,0x3f80,0xbf80,0x3f80,0xbf80,0xbf808000,0x3f808000,0xbf808000,0x3f808000,
0xbe483f80,0x3f0ebf6d,0xbf553f35,0x3f7bbec4,0xbf7b8000,0x3f553ec4,0xbf0ebf35,0x3e483f6d,0xbec43f80,0x3f6dbf35,0xbf6d0000,0x3ec43f35,
0xbf6d8000,0x3ec43f35,0x3ec4bf80,0xbf6d3f35,0xbf0e3f80,0x3f7bbec4,0xbe48bf35,0xbf553f6d,0xbf558000,0xbe483f6d,0x3f7bbf35,0xbf0ebec4,
0xbf353f80,0x3f350000,0x3f35bf80,0xbf350000,0xbf358000,0xbf353f80,0x3f358000,0x3f35bf80,0xbf553f80,0x3e483ec4,0x3f7bbf35,0x3f0ebf6d,
0xbf0e8000,0xbf7b3f6d,0xbe483f35,0x3f55bec4,0xbf6d3f80,0xbec43f35,0x3ec40000,0x3f6dbf35,0xbec48000,0xbf6d3f35,0xbf6d3f80,0xbec43f35,
0xbf7b3f80,0xbf553f6d,0xbf0e3f35,0xbe483ec4,0xbe488000,0xbf0e3ec4,0xbf553f35,0xbf7b3f6d,0x3f803f80,0x3f803f80,0x3f803f80,0x3f803f80,
0x80008000,0x80008000,0x80008000,0x80008000,0xbe480000,0xbf0ebec4,0xbf55bf35,0xbf7bbf6d,0xbf7bbf80,0xbf55bf6d,0xbf0ebf35,0xbe48bec4,
0xbf6dbf80,0xbec4bf35,0x3ec40000,0x3f6d3f35,0x3ec48000,0x3f6d3f35,0x3f6d3f80,0x3ec43f35,0x3f0e0000,0x3f7b3f6d,0x3e483f35,0xbf55bec4,
0x3f553f80,0xbe483ec4,0xbf7bbf35,0xbf0ebf6d,0x3f353f80,0xbf350000,0xbf35bf80,0x3f350000,0xbf358000,0xbf35bf80,0x3f358000,0x3f353f80,
0xbf550000,0xbe48bf6d,0x3f7b3f35,0xbf0e3ec4,0xbf0ebf80,0x3f7b3ec4,0xbe483f35,0xbf55bf6d,0xbec4bf80,0x3f6d3f35,0xbf6d0000,0x3ec4bf35,
0x3f6d8000,0xbec43f35,0xbec4bf80,0x3f6d3f35,0x3f7b0000,0xbf553ec4,0x3f0ebf35,0xbe483f6d,0x3e483f80,0xbf0ebf6d,0x3f553f35,0xbf7bbec4,
0x3f80,0xbf80,0x3f80,0xbf80,0xbf808000,0x3f808000,0xbf808000,0x3f808000,0xbf7b0000,0x3f553ec4,0xbf0ebf35,0x3e483f6d,
0x3e48bf80,0xbf0e3f6d,0x3f55bf35,0xbf7b3ec4,0x3ec4bf80,0xbf6d3f35,0x3f6d0000,0xbec4bf35,0x3f6d8000,0xbec4bf35,0xbec43f80,0x3f6dbf35,
0x3f550000,0x3e48bf6d,0xbf7b3f35,0x3f0e3ec4,0xbf0e3f80,0x3f7bbec4,0xbe48bf35,0xbf553f6d,0xbf353f80,0x3f350000,0x3f35bf80,0xbf350000,
0xbf358000,0xbf353f80,0x3f358000,0x3f35bf80,0xbf0e0000,0xbf7b3f6d,0xbe483f35,0x3f55bec4,0x3f55bf80,0xbe48bec4,0xbf7b3f35,0xbf0e3f6d,
0x3f6dbf80,0x3ec4bf35,0xbec40000,0xbf6d3f35,0x3ec48000,0x3f6dbf35,0x3f6dbf80,0x3ec4bf35,0x3e480000,0x3f0ebec4,0x3f55bf35,0x3f7bbf6d,
0xbf7b3f80,0xbf553f6d,0xbf0e3f35,0xbe483ec4,0x3f803f80,0x3f803f80,0x3f803f80,0x3f803f80,0x80008000,0x80008000,0x80008000,0x80008000,
0xbf7bbf80,0xbf55bf6d,0xbf0ebf35,0xbe48bec4,0x3e488000,0x3f0e3ec4,0x3f553f35,0x3f7b3f6d,0x3f6d3f80,0x3ec43f35,0xbec40000,0xbf6dbf35,
0xbec48000,0xbf6dbf35,0xbf6dbf80,0xbec4bf35,0xbf55bf80,0x3e48bec4,0x3f7b3f35,0x3f0e3f6d,0x3f0e8000,0x3f7b3f6d,0x3e483f35,0xbf55bec4,
0x3f353f80,0xbf350000,0xbf35bf80,0x3f350000,0xbf358000,0xbf35bf80,0x3f358000,0x3f353f80,0xbf0ebf80,0x3f7b3ec4,0xbe483f35,0xbf55bf6d,
0x3f558000,0x3e483f6d,0xbf7bbf35,0x3f0ebec4,0x3ec43f80,0xbf6dbf35,0x3f6d0000,0xbec43f35,0xbf6d8000,0x3ec4bf35,0x3ec43f80,0xbf6dbf35,
0xbe48bf80,0x3f0e3f6d,0xbf55bf35,0x3f7b3ec4,0x3f7b8000,0xbf553ec4,0x3f0ebf35,0xbe483f6d,0x3f80,0xbf80,0x3f80,0xbf80,
0xbf808000,0x3f808000,0xbf808000,0x3f808000,0x3e48bf80,0xbf0e3f6d,0x3f55bf35,0xbf7b3ec4,0x3f7b8000,0xbf55bec4,0x3f0e3f35,0xbe48bf6d,
0xbec43f80,0x3f6dbf35,0xbf6d0000,0x3ec43f35,0xbf6d8000,0x3ec43f35,0x3ec4bf80,0xbf6d3f35,0x3f0ebf80,0xbf7b3ec4,0x3e483f35,0x3f55bf6d,
0x3f558000,0x3e48bf6d,0xbf7b3f35,0x3f0e3ec4,0xbf353f80,0x3f350000,0x3f35bf80,0xbf350000,0xbf358000,0xbf353f80,0x3f358000,0x3f35bf80,
0x3f55bf80,0xbe48bec4,0xbf7b3f35,0xbf0e3f6d,0x3f0e8000,0x3f7bbf6d,0x3e48bf35,0xbf553ec4,0xbf6d3f80,0xbec43f35,0x3ec40000,0x3f6dbf35,
0xbec48000,0xbf6d3f35,0xbf6d3f80,0xbec43f35,0x3f7bbf80,0x3f55bf6d,0x3f0ebf35,0x3e48bec4,0x3e488000,0x3f0ebec4,0x3f55bf35,0x3f7bbf6d,
0x3f803f80,0x3f803f80,0x3f803f80,0x3f803f80,0x80008000,0x80008000,0x80008000,0x80008000,0x3e480000,0x3f0e3ec4,0x3f553f35,0x3f7b3f6d,
0x3f7b3f80,0x3f553f6d,0x3f0e3f35,0x3e483ec4,0xbf6dbf80,0xbec4bf35,0x3ec40000,0x3f6d3f35,0x3ec48000,0x3f6d3f35,0x3f6d3f80,0x3ec43f35,
0xbf0e0000,0xbf7bbf6d,0xbe48bf35,0x3f553ec4,0xbf55bf80,0x3e48bec4,0x3f7b3f35,0x3f0e3f6d,0x3f353f80,0xbf350000,0xbf35bf80,0x3f350000,
0xbf358000,0xbf35bf80,0x3f358000,0x3f353f80,0x3f550000,0x3e483f6d,0xbf7bbf35,0x3f0ebec4,0x3f0e3f80,0xbf7bbec4,0x3e48bf35,0x3f553f6d,
0xbec4bf80,0x3f6d3f35,0xbf6d0000,0x3ec4bf35,0x3f6d8000,0xbec43f35,0xbec4bf80,0x3f6d3f35,0xbf7b0000,0x3f55bec4,0xbf0e3f35,0x3e48bf6d,
0xbe48bf80,0x3f0e3f6d,0xbf55bf35,0x3f7b3ec4,0x3f80,0xbf80,0x3f80,0xbf80,0xbf808000,0x3f808000,0xbf808000,0x3f808000,
0x3f7b0000,0xbf55bec4,0x3f0e3f35,0xbe48bf6d,0xbe483f80,0x3f0ebf6d,0xbf553f35,0x3f7bbec4,0x3ec4bf80,0xbf6d3f35,0x3f6d0000,0xbec4bf35,
0x3f6d8000,0xbec4bf35,0xbec43f80,0x3f6dbf35,0xbf550000,0xbe483f6d,0x3f7bbf35,0xbf0ebec4,0x3f0ebf80,0xbf7b3ec4,0x3e483f35,0x3f55bf6d,
0xbf353f80,0x3f350000,0x3f35bf80,0xbf350000,0xbf358000,0xbf353f80,0x3f358000,0x3f35bf80,0x3f0e0000,0x3f7bbf6d,0x3e48bf35,0xbf553ec4,
0xbf553f80,0x3e483ec4,0x3f7bbf35,0x3f0ebf6d,0x3f6dbf80,0x3ec4bf35,0xbec40000,0xbf6d3f35,0x3ec48000,0x3f6dbf35,0x3f6dbf80,0x3ec4bf35,
0xbe480000,0xbf0e3ec4,0xbf553f35,0xbf7b3f6d,0x3f7bbf80,0x3f55bf6d,0x3f0ebf35,0x3e48bec4,0x0,0x0,0x0,0x0,
0x3f803f80,0x3f803f80,0x3f803f80,0x3f803f80,0x3e480000,0x3f0e3ec4,0x3f553f35,0x3f7b3f6d,0x3f7b3f80,0x3f553f6d,0x3f0e3f35,0x3e483ec4,
0x3ec40000,0x3f6d3f35,0x3f6d3f80,0x3ec43f35,0x3f6d3f80,0x3ec43f35,0xbec40000,0xbf6dbf35,0x3f0e0000,0x3f7b3f6d,0x3e483f35,0xbf55bec4,
0x3f553f80,0xbe483ec4,0xbf7bbf35,0xbf0ebf6d,0x3f350000,0x3f353f80,0xbf350000,0xbf35bf80,0x3f353f80,0xbf350000,0xbf35bf80,0x3f350000,
0x3f550000,0x3e483f6d,0xbf7bbf35,0x3f0ebec4,0x3f0e3f80,0xbf7bbec4,0x3e48bf35,0x3f553f6d,0x3f6d0000,0xbec43f35,0xbec4bf80,0x3f6d3f35,
0x3ec43f80,0xbf6dbf35,0x3f6d0000,0xbec43f35,0x3f7b0000,0xbf553ec4,0x3f0ebf35,0xbe483f6d,0x3e483f80,0xbf0ebf6d,0x3f553f35,0xbf7bbec4,
0x3f800000,0xbf800000,0x3f800000,0xbf800000,0x3f80,0xbf80,0x3f80,0xbf80,0x3f7b0000,0xbf55bec4,0x3f0e3f35,0xbe48bf6d,
0xbe483f80,0x3f0ebf6d,0xbf553f35,0x3f7bbec4,0x3f6d0000,0xbec4bf35,0xbec43f80,0x3f6dbf35,0xbec43f80,0x3f6dbf35,0xbf6d0000,0x3ec43f35,
0x3f550000,0x3e48bf6d,0xbf7b3f35,0x3f0e3ec4,0xbf0e3f80,0x3f7bbec4,0xbe48bf35,0xbf553f6d,0x3f350000,0x3f35bf80,0xbf350000,0xbf353f80,
0xbf353f80,0x3f350000,0x3f35bf80,0xbf350000,0x3f0e0000,0x3f7bbf6d,0x3e48bf35,0xbf553ec4,0xbf553f80,0x3e483ec4,0x3f7bbf35,0x3f0ebf6d,
0x3ec40000,0x3f6dbf35,0x3f6dbf80,0x3ec4bf35,0xbf6d3f80,0xbec43f35,0x3ec40000,0x3f6dbf35,0x3e480000,0x3f0ebec4,0x3f55bf35,0x3f7bbf6d,
0xbf7b3f80,0xbf553f6d,0xbf0e3f35,0xbe483ec4,0x0,0x0,0x0,0x0,0x3f803f80,0x3f803f80,0x3f803f80,0x3f803f80,
0x3f7b3f80,0x3f553f6d,0x3f0e3f35,0x3e483ec4,0xbe480000,0xbf0ebec4,0xbf55bf35,0xbf7bbf6d,0xbec40000,0xbf6dbf35,0xbf6dbf80,0xbec4bf35,
0xbf6dbf80,0xbec4bf35,0x3ec40000,0x3f6d3f35,0xbf55bf80,0x3e48bec4,0x3f7b3f35,0x3f0e3f6d,0x3f0e0000,0x3f7b3f6d,0x3e483f35,0xbf55bec4,
0x3f350000,0x3f353f80,0xbf350000,0xbf35bf80,0x3f353f80,0xbf350000,0xbf35bf80,0x3f350000,0x3f0e3f80,0xbf7bbec4,0x3e48bf35,0x3f553f6d,
0xbf550000,0xbe48bf6d,0x3f7b3f35,0xbf0e3ec4,0xbf6d0000,0x3ec4bf35,0x3ec43f80,0xbf6dbf35,0xbec4bf80,0x3f6d3f35,0xbf6d0000,0x3ec4bf35,
0xbe48bf80,0x3f0e3f6d,0xbf55bf35,0x3f7b3ec4,0x3f7b0000,0xbf553ec4,0x3f0ebf35,0xbe483f6d,0x3f800000,0xbf800000,0x3f800000,0xbf800000,
0x3f80,0xbf80,0x3f80,0xbf80,0xbe483f80,0x3f0ebf6d,0xbf553f35,0x3f7bbec4,0xbf7b0000,0x3f553ec4,0xbf0ebf35,0x3e483f6d,
0xbf6d0000,0x3ec43f35,0x3ec4bf80,0xbf6d3f35,0x3ec4bf80,0xbf6d3f35,0x3f6d0000,0xbec4bf35,0x3f0ebf80,0xbf7b3ec4,0x3e483f35,0x3f55bf6d,
0x3f550000,0x3e48bf6d,0xbf7b3f35,0x3f0e3ec4,0x3f350000,0x3f35bf80,0xbf350000,0xbf353f80,0xbf353f80,0x3f350000,0x3f35bf80,0xbf350000,
0xbf553f80,0x3e483ec4,0x3f7bbf35,0x3f0ebf6d,0xbf0e0000,0xbf7b3f6d,0xbe483f35,0x3f55bec4,0xbec40000,0xbf6d3f35,0xbf6d3f80,0xbec43f35,
0x3f6dbf80,0x3ec4bf35,0xbec40000,0xbf6d3f35,0x3f7bbf80,0x3f55bf6d,0x3f0ebf35,0x3e48bec4,0x3e480000,0x3f0ebec4,0x3f55bf35,0x3f7bbf6d,
0x0,0x0,0x0,0x0,0x3f803f80,0x3f803f80,0x3f803f80,0x3f803f80,0xbe480000,0xbf0ebec4,0xbf55bf35,0xbf7bbf6d,
0xbf7bbf80,0xbf55bf6d,0xbf0ebf35,0xbe48bec4,0x3ec40000,0x3f6d3f35,0x3f6d3f80,0x3ec43f35,0x3f6d3f80,0x3ec43f35,0xbec40000,0xbf6dbf35,
0xbf0e0000,0xbf7bbf6d,0xbe48bf35,0x3f553ec4,0xbf55bf80,0x3e48bec4,0x3f7b3f35,0x3f0e3f6d,0x3f350000,0x3f353f80,0xbf350000,0xbf35bf80,
0x3f353f80,0xbf350000,0xbf35bf80,0x3f350000,0xbf550000,0xbe48bf6d,0x3f7b3f35,0xbf0e3ec4,0xbf0ebf80,0x3f7b3ec4,0xbe483f35,0xbf55bf6d,
0x3f6d0000,0xbec43f35,0xbec4bf80,0x3f6d3f35,0x3ec43f80,0xbf6dbf35,0x3f6d0000,0xbec43f35,0xbf7b0000,0x3f55bec4,0xbf0e3f35,0x3e48bf6d,
0xbe48bf80,0x3f0e3f6d,0xbf55bf35,0x3f7b3ec4,0x3f800000,0xbf800000,0x3f800000,0xbf800000,0x3f80,0xbf80,0x3f80,0xbf80,
0xbf7b0000,0x3f553ec4,0xbf0ebf35,0x3e483f6d,0x3e48bf80,0xbf0e3f6d,0x3f55bf35,0xbf7b3ec4,0x3f6d0000,0xbec4bf35,0xbec43f80,0x3f6dbf35,
0xbec43f80,0x3f6dbf35,0xbf6d0000,0x3ec43f35,0xbf550000,0xbe483f6d,0x3f7bbf35,0xbf0ebec4,0x3f0ebf80,0xbf7b3ec4,0x3e483f35,0x3f55bf6d,
0x3f350000,0x3f35bf80,0xbf350000,0xbf353f80,0xbf353f80,0x3f350000,0x3f35bf80,0xbf350000,0xbf0e0000,0xbf7b3f6d,0xbe483f35,0x3f55bec4,
0x3f55bf80,0xbe48bec4,0xbf7b3f35,0xbf0e3f6d,0x3ec40000,0x3f6dbf35,0x3f6dbf80,0x3ec4bf35,0xbf6d3f80,0xbec43f35,0x3ec40000,0x3f6dbf35,
0xbe480000,0xbf0e3ec4,0xbf553f35,0xbf7b3f6d,0x3f7bbf80,0x3f55bf6d,0x3f0ebf35,0x3e48bec4,0x0,0x0,0x0,0x0,
0x3f803f80,0x3f803f80,0x3f803f80,0x3f803f80,0xbf7bbf80,0xbf55bf6d,0xbf0ebf35,0xbe48bec4,0x3e480000,0x3f0e3ec4,0x3f553f35,0x3f7b3f6d,
0xbec40000,0xbf6dbf35,0xbf6dbf80,0xbec4bf35,0xbf6dbf80,0xbec4bf35,0x3ec40000,0x3f6d3f35,0x3f553f80,0xbe483ec4,0xbf7bbf35,0xbf0ebf6d,
0xbf0e0000,0xbf7bbf6d,0xbe48bf35,0x3f553ec4,0x3f350000,0x3f353f80,0xbf350000,0xbf35bf80,0x3f353f80,0xbf350000,0xbf35bf80,0x3f350000,
0xbf0ebf80,0x3f7b3ec4,0xbe483f35,0xbf55bf6d,0x3f550000,0x3e483f6d,0xbf7bbf35,0x3f0ebec4,0xbf6d0000,0x3ec4bf35,0x3ec43f80,0xbf6dbf35,
0xbec4bf80,0x3f6d3f35,0xbf6d0000,0x3ec4bf35,0x3e483f80,0xbf0ebf6d,0x3f553f35,0xbf7bbec4,0xbf7b0000,0x3f55bec4,0xbf0e3f35,0x3e48bf6d,
0x3f800000,0xbf800000,0x3f800000,0xbf800000,0x3f80,0xbf80,0x3f80,0xbf80,0x3e48bf80,0xbf0e3f6d,0x3f55bf35,0xbf7b3ec4,
0x3f7b0000,0xbf55bec4,0x3f0e3f35,0xbe48bf6d,0xbf6d0000,0x3ec43f35,0x3ec4bf80,0xbf6d3f35,0x3ec4bf80,0xbf6d3f35,0x3f6d0000,0xbec4bf35,
0xbf0e3f80,0x3f7bbec4,0xbe48bf35,0xbf553f6d,0xbf550000,0xbe483f6d,0x3f7bbf35,0xbf0ebec4,0x3f350000,0x3f35bf80,0xbf350000,0xbf353f80,
0xbf353f80,0x3f350000,0x3f35bf80,0xbf350000,0x3f55bf80,0xbe48bec4,0xbf7b3f35,0xbf0e3f6d,0x3f0e0000,0x3f7bbf6d,0x3e48bf35,0xbf553ec4,
0xbec40000,0xbf6d3f35,0xbf6d3f80,0xbec43f35,0x3f6dbf80,0x3ec4bf35,0xbec40000,0xbf6d3f35,0xbf7b3f80,0xbf553f6d,0xbf0e3f35,0xbe483ec4,
0xbe480000,0xbf0e3ec4,0xbf553f35,0xbf7b3f6d,0x3f80,0x3f80,0x3f80,0x3f80,0x3f808000,0x3f808000,0x3f808000,0x3f808000,
0x3f80,0x3e483f7b,0x3ec43f6d,0x3f0e3f55,0x3f808000,0x3f7bbe48,0x3f6dbec4,0x3f55bf0e,0x3f80,0x3ec43f6d,0x3f353f35,0x3f6d3ec4,
0x3f808000,0x3f6dbec4,0x3f35bf35,0x3ec4bf6d,0x3f80,0x3f0e3f55,0x3f6d3ec4,0x3f7bbe48,0x3f808000,0x3f55bf0e,0x3ec4bf6d,0xbe48bf7b,
0x3f80,0x3f353f35,0x3f800000,0x3f35bf35,0x3f808000,0x3f35bf35,0xbf80,0xbf35bf35,0x3f80,0x3f553f0e,0x3f6dbec4,0x3e48bf7b,
0x3f808000,0x3f0ebf55,0xbec4bf6d,0xbf7bbe48,0x3f80,0x3f6d3ec4,0x3f35bf35,0xbec4bf6d,0x3f808000,0x3ec4bf6d,0xbf35bf35,0xbf6d3ec4,
0x3f80,0x3f7b3e48,0x3ec4bf6d,0xbf55bf0e,0x3f808000,0x3e48bf7b,0xbf6dbec4,0xbf0e3f55,0x3f80,0x3f800000,0xbf80,0xbf800000,
0x3f808000,0xbf80,0xbf808000,0x3f80,0x3f80,0x3f7bbe48,0xbec4bf6d,0xbf553f0e,0x3f808000,0xbe48bf7b,0xbf6d3ec4,0x3f0e3f55,
0x3f80,0x3f6dbec4,0xbf35bf35,0xbec43f6d,0x3f808000,0xbec4bf6d,0xbf353f35,0x3f6d3ec4,0x3f80,0x3f55bf0e,0xbf6dbec4,0x3e483f7b,
0x3f808000,0xbf0ebf55,0xbec43f6d,0x3f7bbe48,0x3f80,0x3f35bf35,0xbf800000,0x3f353f35,0x3f808000,0xbf35bf35,0x3f80,0x3f35bf35,
0x3f80,0x3f0ebf55,0xbf6d3ec4,0x3f7b3e48,0x3f808000,0xbf55bf0e,0x3ec43f6d,0x3e48bf7b,0x3f80,0x3ec4bf6d,0xbf353f35,0x3f6dbec4,
0x3f808000,0xbf6dbec4,0x3f353f35,0xbec4bf6d,0x3f80,0x3e48bf7b,0xbec43f6d,0x3f0ebf55,0x3f808000,0xbf7bbe48,0x3f6d3ec4,0xbf55bf0e,
0x3f80,0x3f80,0x3f80,0x3f80,0x3f808000,0x3f808000,0x3f808000,0x3f808000,0x3f353f35,0x3f553f0e,0x3f6d3ec4,0x3f7b3e48,
0x3f35bf35,0x3f0ebf55,0x3ec4bf6d,0x3e48bf7b,0x3f800000,0x3f6dbec4,0x3f35bf35,0x3ec4bf6d,0xbf80,0xbec4bf6d,0xbf35bf35,0xbf6dbec4,
0x3f35bf35,0x3e48bf7b,0xbec4bf6d,0xbf55bf0e,0xbf35bf35,0xbf7bbe48,0xbf6d3ec4,0xbf0e3f55,0xbf80,0xbf35bf35,0xbf800000,0xbf353f35,
0xbf808000,0xbf353f35,0x3f80,0x3f353f35,0xbf35bf35,0xbf7b3e48,0xbec43f6d,0x3f0e3f55,0xbf353f35,0x3e483f7b,0x3f6d3ec4,0x3f55bf0e,
0xbf800000,0xbec43f6d,0x3f353f35,0x3f6dbec4,0x3f80,0x3f6d3ec4,0x3f35bf35,0xbec4bf6d,0xbf353f35,0x3f0e3f55,0x3f6dbec4,0xbe48bf7b,
0x3f353f35,0x3f55bf0e,0xbec4bf6d,0xbf7b3e48,0x3f80,0x3f800000,0xbf80,0xbf800000,0x3f808000,0xbf80,0xbf808000,0x3f80,
0x3f353f35,0x3f0ebf55,0xbf6dbec4,0xbe483f7b,0x3f35bf35,0xbf55bf0e,0xbec43f6d,0x3f7b3e48,0x3f800000,0xbec4bf6d,0xbf353f35,0x3f6d3ec4,
0xbf80,0xbf6d3ec4,0x3f353f35,0x3ec4bf6d,0x3f35bf35,0xbf7bbe48,0x3ec43f6d,0x3f0ebf55,0xbf35bf35,0xbe483f7b,0x3f6dbec4,0xbf55bf0e,
0xbf80,0xbf353f35,0x3f800000,0xbf35bf35,0xbf808000,0x3f353f35,0xbf80,0xbf353f35,0xbf35bf35,0x3e483f7b,0x3ec4bf6d,0xbf553f0e,
0xbf353f35,0x3f7bbe48,0xbf6dbec4,0x3f0e3f55,0xbf800000,0x3f6d3ec4,0xbf35bf35,0x3ec43f6d,0x3f80,0x3ec4bf6d,0xbf353f35,0x3f6dbec4,
0xbf353f35,0x3f55bf0e,0xbf6d3ec4,0x3f7bbe48,0x3f353f35,0xbf0ebf55,0x3ec43f6d,0xbe48bf7b,0x3f80,0x3f80,0x3f80,0x3f80,
0x3f808000,0x3f808000,0x3f808000,0x3f808000,0x3f800000,0x3f7bbe48,0x3f6dbec4,0x3f55bf0e,0xbf80,0xbe48bf7b,0xbec4bf6d,0xbf0ebf55,
0xbf80,0xbec4bf6d,0xbf35bf35,0xbf6dbec4,0xbf808000,0xbf6d3ec4,0xbf353f35,0xbec43f6d,0xbf800000,0xbf553f0e,0xbec43f6d,0x3e483f7b,
0x3f80,0x3f0e3f55,0x3f6d3ec4,0x3f7bbe48,0x3f80,0x3f353f35,0x3f800000,0x3f35bf35,0x3f808000,0x3f35bf35,0xbf80,0xbf35bf35,
0x3f800000,0x3f0ebf55,0xbec4bf6d,0xbf7bbe48,0xbf80,0xbf55bf0e,0xbf6d3ec4,0xbe483f7b,0xbf80,0xbf6dbec4,0xbf353f35,0x3ec43f6d,
0xbf808000,0xbec43f6d,0x3f353f35,0x3f6dbec4,0xbf800000,0xbe483f7b,0x3f6d3ec4,0x3f0ebf55,0x3f80,0x3f7b3e48,0x3ec4bf6d,0xbf55bf0e,
0x3f80,0x3f800000,0xbf80,0xbf800000,0x3f808000,0xbf80,0xbf808000,0x3f80,0x3f800000,0xbe48bf7b,0xbf6d3ec4,0x3f0e3f55,
0xbf80,0xbf7b3e48,0x3ec43f6d,0x3f55bf0e,0xbf80,0xbf6d3ec4,0x3f353f35,0x3ec4bf6d,0xbf808000,0x3ec43f6d,0x3f35bf35,0xbf6dbec4,
0xbf800000,0x3f0e3f55,0x3ec4bf6d,0xbf7b3e48,0x3f80,0x3f55bf0e,0xbf6dbec4,0x3e483f7b,0x3f80,0x3f35bf35,0xbf800000,0x3f353f35,
0x3f808000,0xbf35bf35,0x3f80,0x3f35bf35,0x3f800000,0xbf55bf0e,0x3ec43f6d,0x3e48bf7b,0xbf80,0xbf0e3f55,0x3f6dbec4,0xbf7bbe48,
0xbf80,0xbec43f6d,0x3f35bf35,0xbf6d3ec4,0xbf808000,0x3f6d3ec4,0xbf35bf35,0x3ec43f6d,0xbf800000,0x3f7b3e48,0xbf6dbec4,0x3f553f0e,
0x3f80,0x3e48bf7b,0xbec43f6d,0x3f0ebf55,0x3f80,0x3f80,0x3f80,0x3f80,0x3f808000,0x3f808000,0x3f808000,0x3f808000,
0x3f35bf35,0x3f0ebf55,0x3ec4bf6d,0x3e48bf7b,0xbf35bf35,0xbf55bf0e,0xbf6dbec4,0xbf7bbe48,0xbf800000,0xbf6d3ec4,0xbf353f35,0xbec43f6d,
0x3f80,0x3ec43f6d,0x3f353f35,0x3f6d3ec4,0x3f353f35,0x3f7b3e48,0x3f6dbec4,0x3f0ebf55,0x3f35bf35,0x3e48bf7b,0xbec4bf6d,0xbf55bf0e,
0xbf80,0xbf35bf35,0xbf800000,0xbf353f35,0xbf808000,0xbf353f35,0x3f80,0x3f353f35,0xbf353f35,0x3e483f7b,0x3f6d3ec4,0x3f55bf0e,
0x3f353f35,0x3f7bbe48,0x3ec4bf6d,0xbf0ebf55,0x3f800000,0x3ec4bf6d,0xbf35bf35,0xbf6d3ec4,0xbf80,0xbf6dbec4,0xbf353f35,0x3ec43f6d,
0xbf35bf35,0xbf553f0e,0x3ec43f6d,0x3f7bbe48,0xbf353f35,0x3f0e3f55,0x3f6dbec4,0xbe48bf7b,0x3f80,0x3f800000,0xbf80,0xbf800000,
0x3f808000,0xbf80,0xbf808000,0x3f80,0x3f35bf35,0xbf55bf0e,0xbec43f6d,0x3f7b3e48,0xbf35bf35,0xbf0e3f55,0x3f6d3ec4,0x3e48bf7b,
0xbf800000,0x3ec43f6d,0x3f35bf35,0xbf6dbec4,0x3f80,0x3f6dbec4,0xbf35bf35,0xbec43f6d,0x3f353f35,0x3e48bf7b,0xbf6d3ec4,0x3f553f0e,
0x3f35bf35,0xbf7bbe48,0x3ec43f6d,0x3f0ebf55,0xbf80,0xbf353f35,0x3f800000,0xbf35bf35,0xbf808000,0x3f353f35,0xbf80,0xbf353f35,
0xbf353f35,0x3f7bbe48,0xbf6dbec4,0x3f0e3f55,0x3f353f35,0xbe48bf7b,0xbec43f6d,0x3f55bf0e,0x3f800000,0xbf6dbec4,0x3f353f35,0xbec4bf6d,
0xbf80,0xbec43f6d,0x3f35bf35,0xbf6d3ec4,0xbf35bf35,0x3f0e3f55,0xbec4bf6d,0x3e483f7b,0xbf353f35,0x3f55bf0e,0xbf6d3ec4,0x3f7bbe48,
0x3f80,0x3f80,0x3f80,0x3f80,0x3f808000,0x3f808000,0x3f808000,0x3f808000,0xbf80,0xbe48bf7b,0xbec4bf6d,0xbf0ebf55,
0xbf808000,0xbf7b3e48,0xbf6d3ec4,0xbf553f0e,0x3f80,0x3ec43f6d,0x3f353f35,0x3f6d3ec4,0x3f808000,0x3f6dbec4,0x3f35bf35,0x3ec4bf6d,
0xbf80,0xbf0ebf55,0xbf6dbec4,0xbf7b3e48,0xbf808000,0xbf553f0e,0xbec43f6d,0x3e483f7b,0x3f80,0x3f353f35,0x3f800000,0x3f35bf35,
0x3f808000,0x3f35bf35,0xbf80,0xbf35bf35,0xbf80,0xbf55bf0e,0xbf6d3ec4,0xbe483f7b,0xbf808000,0xbf0e3f55,0x3ec43f6d,0x3f7b3e48,
0x3f80,0x3f6d3ec4,0x3f35bf35,0xbec4bf6d,0x3f808000,0x3ec4bf6d,0xbf35bf35,0xbf6d3ec4,0xbf80,0xbf7bbe48,0xbec43f6d,0x3f553f0e,
0xbf808000,0xbe483f7b,0x3f6d3ec4,0x3f0ebf55,0x3f80,0x3f800000,0xbf80,0xbf800000,0x3f808000,0xbf80,0xbf808000,0x3f80,
0xbf80,0xbf7b3e48,0x3ec43f6d,0x3f55bf0e,0xbf808000,0x3e483f7b,0x3f6dbec4,0xbf0ebf55,0x3f80,0x3f6dbec4,0xbf35bf35,0xbec43f6d,
0x3f808000,0xbec4bf6d,0xbf353f35,0x3f6d3ec4,0xbf80,0xbf553f0e,0x3f6d3ec4,0xbe48bf7b,0xbf808000,0x3f0e3f55,0x3ec4bf6d,0xbf7b3e48,
0x3f80,0x3f35bf35,0xbf800000,0x3f353f35,0x3f808000,0xbf35bf35,0x3f80,0x3f35bf35,0xbf80,0xbf0e3f55,0x3f6dbec4,0xbf7bbe48,
0xbf808000,0x3f553f0e,0xbec4bf6d,0xbe483f7b,0x3f80,0x3ec4bf6d,0xbf353f35,0x3f6dbec4,0x3f808000,0xbf6dbec4,0x3f353f35,0xbec4bf6d,
0xbf80,0xbe483f7b,0x3ec4bf6d,0xbf0e3f55,0xbf808000,0x3f7b3e48,0xbf6dbec4,0x3f553f0e,0x3f80,0x3f80,0x3f80,0x3f80,
0x3f808000,0x3f808000,0x3f808000,0x3f808000,0xbf35bf35,0xbf55bf0e,0xbf6dbec4,0xbf7bbe48,0xbf353f35,0xbf0e3f55,0xbec43f6d,0xbe483f7b,
0x3f800000,0x3f6dbec4,0x3f35bf35,0x3ec4bf6d,0xbf80,0xbec4bf6d,0xbf35bf35,0xbf6dbec4,0xbf353f35,0xbe483f7b,0x3ec43f6d,0x3f553f0e,
0x3f353f35,0x3f7b3e48,0x3f6dbec4,0x3f0ebf55,0xbf80,0xbf35bf35,0xbf800000,0xbf353f35,0xbf808000,0xbf353f35,0x3f80,0x3f353f35,
0x3f353f35,0x3f7bbe48,0x3ec4bf6d,0xbf0ebf55,0x3f35bf35,0xbe48bf7b,0xbf6dbec4,0xbf553f0e,0xbf800000,0xbec43f6d,0x3f353f35,0x3f6dbec4,
0x3f80,0x3f6d3ec4,0x3f35bf35,0xbec4bf6d,0x3f35bf35,0xbf0ebf55,0xbf6d3ec4,0x3e483f7b,0xbf35bf35,0xbf553f0e,0x3ec43f6d,0x3f7bbe48,
0x3f80,0x3f800000,0xbf80,0xbf800000,0x3f808000,0xbf80,0xbf808000,0x3f80,0xbf35bf35,0xbf0e3f55,0x3f6d3ec4,0x3e48bf7b,
0xbf353f35,0x3f553f0e,0x3ec4bf6d,0xbf7bbe48,0x3f800000,0xbec4bf6d,0xbf353f35,0x3f6d3ec4,0xbf80,0xbf6d3ec4,0x3f353f35,0x3ec4bf6d,
0xbf353f35,0x3f7b3e48,0xbec4bf6d,0xbf0e3f55,0x3f353f35,0x3e48bf7b,0xbf6d3ec4,0x3f553f0e,0xbf80,0xbf353f35,0x3f800000,0xbf35bf35,
0xbf808000,0x3f353f35,0xbf80,0xbf353f35,0x3f353f35,0xbe48bf7b,0xbec43f6d,0x3f55bf0e,0x3f35bf35,0xbf7b3e48,0x3f6d3ec4,0xbf0ebf55,
0xbf800000,0x3f6d3ec4,0xbf35bf35,0x3ec43f6d,0x3f80,0x3ec4bf6d,0xbf353f35,0x3f6dbec4,0x3f35bf35,0xbf553f0e,0x3f6dbec4,0xbf7b3e48,
0xbf35bf35,0x3f0e3f55,0xbec4bf6d,0x3e483f7b,0x3f80,0x3f80,0x3f80,0x3f80,0x3f808000,0x3f808000,0x3f808000,0x3f808000,
0xbf800000,0xbf7b3e48,0xbf6d3ec4,0xbf553f0e,0x3f80,0x3e483f7b,0x3ec43f6d,0x3f0e3f55,0xbf80,0xbec4bf6d,0xbf35bf35,0xbf6dbec4,
0xbf808000,0xbf6d3ec4,0xbf353f35,0xbec43f6d,0x3f800000,0x3f55bf0e,0x3ec4bf6d,0xbe48bf7b,0xbf80,0xbf0ebf55,0xbf6dbec4,0xbf7b3e48,
0x3f80,0x3f353f35,0x3f800000,0x3f35bf35,0x3f808000,0x3f35bf35,0xbf80,0xbf35bf35,0xbf800000,0xbf0e3f55,0x3ec43f6d,0x3f7b3e48,
0x3f80,0x3f553f0e,0x3f6dbec4,0x3e48bf7b,0xbf80,0xbf6dbec4,0xbf353f35,0x3ec43f6d,0xbf808000,0xbec43f6d,0x3f353f35,0x3f6dbec4,
0x3f800000,0x3e48bf7b,0xbf6dbec4,0xbf0e3f55,0xbf80,0xbf7bbe48,0xbec43f6d,0x3f553f0e,0x3f80,0x3f800000,0xbf80,0xbf800000,
0x3f808000,0xbf80,0xbf808000,0x3f80,0xbf800000,0x3e483f7b,0x3f6dbec4,0xbf0ebf55,0x3f80,0x3f7bbe48,0xbec4bf6d,0xbf553f0e,
0xbf80,0xbf6d3ec4,0x3f353f35,0x3ec4bf6d,0xbf808000,0x3ec43f6d,0x3f35bf35,0xbf6dbec4,0x3f800000,0xbf0ebf55,0xbec43f6d,0x3f7bbe48,
0xbf80,0xbf553f0e,0x3f6d3ec4,0xbe48bf7b,0x3f80,0x3f35bf35,0xbf800000,0x3f353f35,0x3f808000,0xbf35bf35,0x3f80,0x3f35bf35,
0xbf800000,0x3f553f0e,0xbec4bf6d,0xbe483f7b,0x3f80,0x3f0ebf55,0xbf6d3ec4,0x3f7b3e48,0xbf80,0xbec43f6d,0x3f35bf35,0xbf6d3ec4,
0xbf808000,0x3f6d3ec4,0xbf35bf35,0x3ec43f6d,0x3f800000,0xbf7bbe48,0x3f6d3ec4,0xbf55bf0e,0xbf80,0xbe483f7b,0x3ec4bf6d,0xbf0e3f55,
0x3f80,0x3f80,0x3f80,0x3f80,0x3f808000,0x3f808000,0x3f808000,0x3f808000,0xbf353f35,0xbf0e3f55,0xbec43f6d,0xbe483f7b,
0x3f353f35,0x3f553f0e,0x3f6d3ec4,0x3f7b3e48,0xbf800000,0xbf6d3ec4,0xbf353f35,0xbec43f6d,0x3f80,0x3ec43f6d,0x3f353f35,0x3f6d3ec4,
0xbf35bf35,0xbf7bbe48,0xbf6d3ec4,0xbf0e3f55,0xbf353f35,0xbe483f7b,0x3ec43f6d,0x3f553f0e,0xbf80,0xbf35bf35,0xbf800000,0xbf353f35,
0xbf808000,0xbf353f35,0x3f80,0x3f353f35,0x3f35bf35,0xbe48bf7b,0xbf6dbec4,0xbf553f0e,0xbf35bf35,0xbf7b3e48,0xbec43f6d,0x3f0e3f55,
0x3f800000,0x3ec4bf6d,0xbf35bf35,0xbf6d3ec4,0xbf80,0xbf6dbec4,0xbf353f35,0x3ec43f6d,0x3f353f35,0x3f55bf0e,0xbec4bf6d,0xbf7b3e48,
0x3f35bf35,0xbf0ebf55,0xbf6d3ec4,0x3e483f7b,0x3f80,0x3f800000,0xbf80,0xbf800000,0x3f808000,0xbf80,0xbf808000,0x3f80,
0xbf353f35,0x3f553f0e,0x3ec4bf6d,0xbf7bbe48,0x3f353f35,0x3f0ebf55,0xbf6dbec4,0xbe483f7b,0xbf800000,0x3ec43f6d,0x3f35bf35,0xbf6dbec4,
0x3f80,0x3f6dbec4,0xbf35bf35,0xbec43f6d,0xbf35bf35,0xbe483f7b,0x3f6dbec4,0xbf55bf0e,0xbf353f35,0x3f7b3e48,0xbec4bf6d,0xbf0e3f55,
0xbf80,0xbf353f35,0x3f800000,0xbf35bf35,0xbf808000,0x3f353f35,0xbf80,0xbf353f35,0x3f35bf35,0xbf7b3e48,0x3f6d3ec4,0xbf0ebf55,
0xbf35bf35,0x3e483f7b,0x3ec4bf6d,0xbf553f0e,0x3f800000,0xbf6dbec4,0x3f353f35,0xbec4bf6d,0xbf80,0xbec43f6d,0x3f35bf35,0xbf6d3ec4,
0x3f353f35,0xbf0ebf55,0x3ec43f6d,0xbe48bf7b,0x3f35bf35,0xbf553f0e,0x3f6dbec4,0xbf7b3e48,
};
__device__ const unsigned __attribute__((aligned(256))) TAB_P2[4096] = {
0x3f80,0x3f80,0x3f80,0x3f80,0x3f808000,0x3f808000,0x3f808000,0x3f808000,0x3f80,0x3dc93f7f,0x3f353f35,0x3f463f22,
0x3f808000,0x3f7fbdc9,0x3f35bf35,0x3f22bf46,0x3f80,0x3e483f7b,0x3f800000,0x3f7bbe48,0x3f808000,0x3f7bbe48,0xbf80,0xbe48bf7b,
0x3f80,0x3e953f75,0x3f35bf35,0x3ef1bf62,0x3f808000,0x3f75be95,0xbf35bf35,0xbf62bef1,0x3f80,0x3ec43f6d,0xbf80,0xbec4bf6d,
0x3f808000,0x3f6dbec4,0xbf808000,0xbf6d3ec4,0x3f80,0x3ef13f62,0xbf35bf35,0xbf75be95,0x3f808000,0x3f62bef1,0xbf353f35,0xbe953f75,
0x3f80,0x3f0e3f55,0xbf800000,0xbf553f0e,0x3f808000,0x3f55bf0e,0x3f80,0x3f0e3f55,0x3f80,0x3f223f46,0xbf353f35,0xbdc93f7f,
0x3f808000,0x3f46bf22,0x3f353f35,0x3f7f3dc9,0x3f80,0x3f80,0x3f80,0x3f80,0x3f808000,0x3f808000,0x3f808000,0x3f808000,
0x3e483f7b,0x3e953f75,0x3f553f0e,0x3f623ef1,0x3f7bbe48,0x3f75be95,0x3f0ebf55,0x3ef1bf62,0x3ec43f6d,0x3f0e3f55,0x3f6dbec4,0x3f55bf0e,
0x3f6dbec4,0x3f55bf0e,0xbec4bf6d,0xbf0ebf55,0x3f0e3f55,0x3f463f22,0x3e48bf7b,0xbdc9bf7f,0x3f55bf0e,0x3f22bf46,0xbf7bbe48,0xbf7f3dc9,
0x3f353f35,0x3f6d3ec4,0xbf35bf35,0xbf6dbec4,0x3f35bf35,0x3ec4bf6d,0xbf353f35,0xbec43f6d,0x3f553f0e,0x3f7f3dc9,0xbf7b3e48,0xbf463f22,
0x3f0ebf55,0x3dc9bf7f,0x3e483f7b,0x3f223f46,0x3f6d3ec4,0x3f7bbe48,0xbec43f6d,0x3e483f7b,0x3ec4bf6d,0xbe48bf7b,0x3f6d3ec4,0x3f7bbe48,
0x3f7b3e48,0x3f62bef1,0x3f0e3f55,0x3f753e95,0x3e48bf7b,0xbef1bf62,0x3f55bf0e,0x3e95bf75,0x3f80,0x3f80,0x3f80,0x3f80,
0x3f808000,0x3f808000,0x3f808000,0x3f808000,0x3ec43f6d,0x3ef13f62,0x3f6d3ec4,0x3f753e95,0x3f6dbec4,0x3f62bef1,0x3ec4bf6d,0x3e95bf75,
0x3f353f35,0x3f553f0e,0x3f35bf35,0x3f0ebf55,0x3f35bf35,0x3f0ebf55,0xbf35bf35,0xbf55bf0e,0x3f6d3ec4,0x3f7f3dc9,0xbec4bf6d,0xbf22bf46,
0x3ec4bf6d,0x3dc9bf7f,0xbf6d3ec4,0xbf463f22,0x3f800000,0x3f6dbec4,0xbf800000,0xbf6d3ec4,0xbf80,0xbec4bf6d,0x3f80,0x3ec43f6d,
0x3f6dbec4,0x3f22bf46,0xbec43f6d,0x3dc93f7f,0xbec4bf6d,0xbf46bf22,0x3f6d3ec4,0x3f7fbdc9,0x3f35bf35,0x3e48bf7b,0x3f353f35,0x3f7b3e48,
0xbf35bf35,0xbf7bbe48,0x3f35bf35,0x3e48bf7b,0x3ec4bf6d,0xbe95bf75,0x3f6dbec4,0x3ef1bf62,0xbf6dbec4,0xbf753e95,0xbec4bf6d,0xbf62bef1,
0x3f80,0x3f80,0x3f80,0x3f80,0x3f808000,0x3f808000,0x3f808000,0x3f808000,0x3f0e3f55,0x3f223f46,0x3f7b3e48,0x3f7f3dc9,
0x3f55bf0e,0x3f46bf22,0x3e48bf7b,0x3dc9bf7f,0x3f6d3ec4,0x3f7b3e48,0x3ec4bf6d,0x3e48bf7b,0x3ec4bf6d,0x3e48bf7b,0xbf6dbec4,0xbf7bbe48,
0x3f7bbe48,0x3f62bef1,0xbf55bf0e,0xbf75be95,0xbe48bf7b,0xbef1bf62,0xbf0e3f55,0xbe953f75,0x3f35bf35,0x3ec4bf6d,0xbf353f35,0xbec43f6d,
0xbf35bf35,0xbf6dbec4,0x3f353f35,0x3f6d3ec4,0x3e48bf7b,0xbe95bf75,0x3f0e3f55,0x3f623ef1,0xbf7bbe48,0xbf753e95,0x3f55bf0e,0x3ef1bf62,
0xbec4bf6d,0xbf55bf0e,0x3f6dbec4,0x3f0ebf55,0xbf6d3ec4,0xbf0e3f55,0xbec4bf6d,0xbf55bf0e,0xbf55bf0e,0xbf7f3dc9,0xbe48bf7b,0xbf46bf22,
0xbf0e3f55,0x3dc93f7f,0xbf7b3e48,0xbf223f46,0x3f80,0x3f80,0x3f80,0x3f80,0x3f808000,0x3f808000,0x3f808000,0x3f808000,
0x3f800000,0x3f7fbdc9,0x3f35bf35,0x3f22bf46,0xbf80,0xbdc9bf7f,0xbf35bf35,0xbf46bf22,0xbf80,0xbe48bf7b,0xbf800000,0xbf7b3e48,
0xbf808000,0xbf7b3e48,0x3f80,0x3e483f7b,0xbf800000,0xbf753e95,0x3f353f35,0x3f623ef1,0x3f80,0x3e953f75,0x3f35bf35,0x3ef1bf62,
0x3f80,0x3ec43f6d,0xbf80,0xbec4bf6d,0x3f808000,0x3f6dbec4,0xbf808000,0xbf6d3ec4,0x3f800000,0x3f62bef1,0xbf353f35,0xbe953f75,
0xbf80,0xbef1bf62,0x3f353f35,0x3f753e95,0xbf80,0xbf0ebf55,0x3f800000,0x3f55bf0e,0xbf808000,0xbf553f0e,0xbf80,0xbf0ebf55,
0xbf800000,0xbf463f22,0xbf35bf35,0xbf7fbdc9,0x3f80,0x3f223f46,0xbf353f35,0xbdc93f7f,0x3f80,0x3f80,0x3f80,0x3f80,
0x3f808000,0x3f808000,0x3f808000,0x3f808000,0x3f7bbe48,0x3f75be95,0x3f0ebf55,0x3ef1bf62,0xbe48bf7b,0xbe95bf75,0xbf55bf0e,0xbf62bef1,
0xbec4bf6d,0xbf0ebf55,0xbf6d3ec4,0xbf553f0e,0xbf6d3ec4,0xbf553f0e,0x3ec43f6d,0x3f0e3f55,0xbf553f0e,0xbf223f46,0x3f7b3e48,0x3f7fbdc9,
0x3f0e3f55,0x3f463f22,0x3e48bf7b,0xbdc9bf7f,0x3f353f35,0x3f6d3ec4,0xbf35bf35,0xbf6dbec4,0x3f35bf35,0x3ec4bf6d,0xbf353f35,0xbec43f6d,
0x3f0ebf55,0x3dc9bf7f,0x3e483f7b,0x3f223f46,0xbf55bf0e,0xbf7fbdc9,0x3f7bbe48,0x3f46bf22,0xbf6dbec4,0xbf7b3e48,0x3ec4bf6d,0xbe48bf7b,
0xbec43f6d,0x3e483f7b,0xbf6dbec4,0xbf7b3e48,0xbe483f7b,0x3ef13f62,0xbf553f0e,0xbe953f75,0x3f7b3e48,0x3f62bef1,0x3f0e3f55,0x3f753e95,
0x3f80,0x3f80,0x3f80,0x3f80,0x3f808000,0x3f808000,0x3f808000,0x3f808000,0x3f6dbec4,0x3f62bef1,0x3ec4bf6d,0x3e95bf75,
0xbec4bf6d,0xbef1bf62,0xbf6dbec4,0xbf75be95,0xbf35bf35,0xbf55bf0e,0xbf353f35,0xbf0e3f55,0xbf353f35,0xbf0e3f55,0x3f353f35,0x3f553f0e,
0xbec43f6d,0xbdc93f7f,0x3f6dbec4,0x3f46bf22,0x3f6d3ec4,0x3f7f3dc9,0xbec4bf6d,0xbf22bf46,0x3f800000,0x3f6dbec4,0xbf800000,0xbf6d3ec4,
0xbf80,0xbec4bf6d,0x3f80,0x3ec43f6d,0xbec4bf6d,0xbf46bf22,0x3f6d3ec4,0x3f7fbdc9,0xbf6d3ec4,0xbf223f46,0x3ec4bf6d,0xbdc9bf7f,
0xbf353f35,0xbe483f7b,0xbf35bf35,0xbf7bbe48,0x3f353f35,0x3f7b3e48,0xbf353f35,0xbe483f7b,0x3f6d3ec4,0x3f75be95,0x3ec43f6d,0x3f623ef1,
0x3ec4bf6d,0xbe95bf75,0x3f6dbec4,0x3ef1bf62,0x3f80,0x3f80,0x3f80,0x3f80,0x3f808000,0x3f808000,0x3f808000,0x3f808000,
0x3f55bf0e,0x3f46bf22,0x3e48bf7b,0x3dc9bf7f,0xbf0ebf55,0xbf22bf46,0xbf7bbe48,0xbf7fbdc9,0xbf6dbec4,0xbf7bbe48,0xbec43f6d,0xbe483f7b,
0xbec43f6d,0xbe483f7b,0x3f6d3ec4,0x3f7b3e48,0x3e483f7b,0x3ef13f62,0x3f0ebf55,0x3e95bf75,0x3f7bbe48,0x3f62bef1,0xbf55bf0e,0xbf75be95,
0x3f35bf35,0x3ec4bf6d,0xbf353f35,0xbec43f6d,0xbf35bf35,0xbf6dbec4,0x3f353f35,0x3f6d3ec4,0xbf7bbe48,0xbf753e95,0x3f55bf0e,0x3ef1bf62,
0xbe483f7b,0x3e953f75,0xbf0ebf55,0xbf62bef1,0x3ec43f6d,0x3f553f0e,0xbf6d3ec4,0xbf0e3f55,0x3f6dbec4,0x3f0ebf55,0x3ec43f6d,0x3f553f0e,
0x3f0ebf55,0xbdc9bf7f,0x3f7bbe48,0x3f22bf46,0xbf55bf0e,0xbf7f3dc9,0xbe48bf7b,0xbf46bf22,0x3f80,0x3f80,0x3f80,0x3f80,
0x3f808000,0x3f808000,0x3f808000,0x3f808000,0xbf80,0xbdc9bf7f,0xbf35bf35,0xbf46bf22,0xbf808000,0xbf7f3dc9,0xbf353f35,0xbf223f46,
0x3f80,0x3e483f7b,0x3f800000,0x3f7bbe48,0x3f808000,0x3f7bbe48,0xbf80,0xbe48bf7b,0xbf80,0xbe95bf75,0xbf353f35,0xbef13f62,
0xbf808000,0xbf753e95,0x3f353f35,0x3f623ef1,0x3f80,0x3ec43f6d,0xbf80,0xbec4bf6d,0x3f808000,0x3f6dbec4,0xbf808000,0xbf6d3ec4,
0xbf80,0xbef1bf62,0x3f353f35,0x3f753e95,0xbf808000,0xbf623ef1,0x3f35bf35,0x3e95bf75,0x3f80,0x3f0e3f55,0xbf800000,0xbf553f0e,
0x3f808000,0x3f55bf0e,0x3f80,0x3f0e3f55,0xbf80,0xbf22bf46,0x3f35bf35,0x3dc9bf7f,0xbf808000,0xbf463f22,0xbf35bf35,0xbf7fbdc9,
0x3f80,0x3f80,0x3f80,0x3f80,0x3f808000,0x3f808000,0x3f808000,0x3f808000,0xbe48bf7b,0xbe95bf75,0xbf55bf0e,0xbf62bef1,
0xbf7b3e48,0xbf753e95,0xbf0e3f55,0xbef13f62,0x3ec43f6d,0x3f0e3f55,0x3f6dbec4,0x3f55bf0e,0x3f6dbec4,0x3f55bf0e,0xbec4bf6d,0xbf0ebf55,
0xbf0ebf55,0xbf46bf22,0xbe483f7b,0x3dc93f7f,0xbf553f0e,0xbf223f46,0x3f7b3e48,0x3f7fbdc9,0x3f353f35,0x3f6d3ec4,0xbf35bf35,0xbf6dbec4,
0x3f35bf35,0x3ec4bf6d,0xbf353f35,0xbec43f6d,0xbf55bf0e,0xbf7fbdc9,0x3f7bbe48,0x3f46bf22,0xbf0e3f55,0xbdc93f7f,0xbe48bf7b,0xbf22bf46,
0x3f6d3ec4,0x3f7bbe48,0xbec43f6d,0x3e483f7b,0x3ec4bf6d,0xbe48bf7b,0x3f6d3ec4,0x3f7bbe48,0xbf7bbe48,0xbf623ef1,0xbf0ebf55,0xbf75be95,
0xbe483f7b,0x3ef13f62,0xbf553f0e,0xbe953f75,0x3f80,0x3f80,0x3f80,0x3f80,0x3f808000,0x3f808000,0x3f808000,0x3f808000,
0xbec4bf6d,0xbef1bf62,0xbf6dbec4,0xbf75be95,0xbf6d3ec4,0xbf623ef1,0xbec43f6d,0xbe953f75,0x3f353f35,0x3f553f0e,0x3f35bf35,0x3f0ebf55,
0x3f35bf35,0x3f0ebf55,0xbf35bf35,0xbf55bf0e,0xbf6dbec4,0xbf7fbdc9,0x3ec43f6d,0x3f223f46,0xbec43f6d,0xbdc93f7f,0x3f6dbec4,0x3f46bf22,
0x3f800000,0x3f6dbec4,0xbf800000,0xbf6d3ec4,0xbf80,0xbec4bf6d,0x3f80,0x3ec43f6d,0xbf6d3ec4,0xbf223f46,0x3ec4bf6d,0xbdc9bf7f,
0x3ec43f6d,0x3f463f22,0xbf6dbec4,0xbf7f3dc9,0x3f35bf35,0x3e48bf7b,0x3f353f35,0x3f7b3e48,0xbf35bf35,0xbf7bbe48,0x3f35bf35,0x3e48bf7b,
0xbec43f6d,0x3e953f75,0xbf6d3ec4,0xbef13f62,0x3f6d3ec4,0x3f75be95,0x3ec43f6d,0x3f623ef1,0x3f80,0x3f80,0x3f80,0x3f80,
0x3f808000,0x3f808000,0x3f808000,0x3f808000,0xbf0ebf55,0xbf22bf46,0xbf7bbe48,0xbf7fbdc9,0xbf553f0e,0xbf463f22,0xbe483f7b,0xbdc93f7f,
0x3f6d3ec4,0x3f7b3e48,0x3ec4bf6d,0x3e48bf7b,0x3ec4bf6d,0x3e48bf7b,0xbf6dbec4,0xbf7bbe48,0xbf7b3e48,0xbf623ef1,0x3f553f0e,0x3f753e95,
0x3e483f7b,0x3ef13f62,0x3f0ebf55,0x3e95bf75,0x3f35bf35,0x3ec4bf6d,0xbf353f35,0xbec43f6d,0xbf35bf35,0xbf6dbec4,0x3f353f35,0x3f6d3ec4,
0xbe483f7b,0x3e953f75,0xbf0ebf55,0xbf62bef1,0x3f7b3e48,0x3f75be95,0xbf553f0e,0xbef13f62,0xbec4bf6d,0xbf55bf0e,0x3f6dbec4,0x3f0ebf55,
0xbf6d3ec4,0xbf0e3f55,0xbec4bf6d,0xbf55bf0e,0x3f553f0e,0x3f7fbdc9,0x3e483f7b,0x3f463f22,0x3f0ebf55,0xbdc9bf7f,0x3f7bbe48,0x3f22bf46,
0x3f80,0x3f80,0x3f80,0x3f80,0x3f808000,0x3f808000,0x3f808000,0x3f808000,0xbf800000,0xbf7f3dc9,0xbf353f35,0xbf223f46,
0x3f80,0x3dc93f7f,0x3f353f35,0x3f463f22,0xbf80,0xbe48bf7b,0xbf800000,0xbf7b3e48,0xbf808000,0xbf7b3e48,0x3f80,0x3e483f7b,
0x3f800000,0x3f75be95,0xbf35bf35,0xbf62bef1,0xbf80,0xbe95bf75,0xbf353f35,0xbef13f62,0x3f80,0x3ec43f6d,0xbf80,0xbec4bf6d,
0x3f808000,0x3f6dbec4,0xbf808000,0xbf6d3ec4,0xbf800000,0xbf623ef1,0x3f35bf35,0x3e95bf75,0x3f80,0x3ef13f62,0xbf35bf35,0xbf75be95,
0xbf80,0xbf0ebf55,0x3f800000,0x3f55bf0e,0xbf808000,0xbf553f0e,0xbf80,0xbf0ebf55,0x3f800000,0x3f46bf22,0x3f353f35,0x3f7f3dc9,
0xbf80,0xbf22bf46,0x3f35bf35,0x3dc9bf7f,0x3f80,0x3f80,0x3f80,0x3f80,0x3f808000,0x3f808000,0x3f808000,0x3f808000,
0xbf7b3e48,0xbf753e95,0xbf0e3f55,0xbef13f62,0x3e483f7b,0x3e953f75,0x3f553f0e,0x3f623ef1,0xbec4bf6d,0xbf0ebf55,0xbf6d3ec4,0xbf553f0e,
0xbf6d3ec4,0xbf553f0e,0x3ec43f6d,0x3f0e3f55,0x3f55bf0e,0x3f22bf46,0xbf7bbe48,0xbf7f3dc9,0xbf0ebf55,0xbf46bf22,0xbe483f7b,0x3dc93f7f,
0x3f353f35,0x3f6d3ec4,0xbf35bf35,0xbf6dbec4,0x3f35bf35,0x3ec4bf6d,0xbf353f35,0xbec43f6d,0xbf0e3f55,0xbdc93f7f,0xbe48bf7b,0xbf22bf46,
0x3f553f0e,0x3f7f3dc9,0xbf7b3e48,0xbf463f22,0xbf6dbec4,0xbf7b3e48,0x3ec4bf6d,0xbe48bf7b,0xbec43f6d,0x3e483f7b,0xbf6dbec4,0xbf7b3e48,
0x3e48bf7b,0xbef1bf62,0x3f55bf0e,0x3e95bf75,0xbf7bbe48,0xbf623ef1,0xbf0ebf55,0xbf75be95,0x3f80,0x3f80,0x3f80,0x3f80,
0x3f808000,0x3f808000,0x3f808000,0x3f808000,0xbf6d3ec4,0xbf623ef1,0xbec43f6d,0xbe953f75,0x3ec43f6d,0x3ef13f62,0x3f6d3ec4,0x3f753e95,
0xbf35bf35,0xbf55bf0e,0xbf353f35,0xbf0e3f55,0xbf353f35,0xbf0e3f55,0x3f353f35,0x3f553f0e,0x3ec4bf6d,0x3dc9bf7f,0xbf6d3ec4,0xbf463f22,
0xbf6dbec4,0xbf7fbdc9,0x3ec43f6d,0x3f223f46,0x3f800000,0x3f6dbec4,0xbf800000,0xbf6d3ec4,0xbf80,0xbec4bf6d,0x3f80,0x3ec43f6d,
0x3ec43f6d,0x3f463f22,0xbf6dbec4,0xbf7f3dc9,0x3f6dbec4,0x3f22bf46,0xbec43f6d,0x3dc93f7f,0xbf353f35,0xbe483f7b,0xbf35bf35,0xbf7bbe48,
0x3f353f35,0x3f7b3e48,0xbf353f35,0xbe483f7b,0xbf6dbec4,0xbf753e95,0xbec4bf6d,0xbf62bef1,0xbec43f6d,0x3e953f75,0xbf6d3ec4,0xbef13f62,
0x3f80,0x3f80,0x3f80,0x3f80,0x3f808000,0x3f808000,0x3f808000,0x3f808000,0xbf553f0e,0xbf463f22,0xbe483f7b,0xbdc93f7f,
0x3f0e3f55,0x3f223f46,0x3f7b3e48,0x3f7f3dc9,0xbf6dbec4,0xbf7bbe48,0xbec43f6d,0xbe483f7b,0xbec43f6d,0xbe483f7b,0x3f6d3ec4,0x3f7b3e48,
0xbe48bf7b,0xbef1bf62,0xbf0e3f55,0xbe953f75,0xbf7b3e48,0xbf623ef1,0x3f553f0e,0x3f753e95,0x3f35bf35,0x3ec4bf6d,0xbf353f35,0xbec43f6d,
0xbf35bf35,0xbf6dbec4,0x3f353f35,0x3f6d3ec4,0x3f7b3e48,0x3f75be95,0xbf553f0e,0xbef13f62,0x3e48bf7b,0xbe95bf75,0x3f0e3f55,0x3f623ef1,
0x3ec43f6d,0x3f553f0e,0xbf6d3ec4,0xbf0e3f55,0x3f6dbec4,0x3f0ebf55,0x3ec43f6d,0x3f553f0e,0xbf0e3f55,0x3dc93f7f,0xbf7b3e48,0xbf223f46,
0x3f553f0e,0x3f7fbdc9,0x3e483f7b,0x3f463f22,0x3f80,0x3f353f35,0x3f80,0x3f353f35,0x3f808000,0x3f35bf35,0x3f808000,0x3f35bf35,
0x3f80,0x3f463f22,0x3f353f35,0x3f7fbdc9,0x3f808000,0x3f22bf46,0x3f35bf35,0xbdc9bf7f,0x3f80,0x3f553f0e,0x3f800000,0x3f0ebf55,
0x3f808000,0x3f0ebf55,0xbf80,0xbf55bf0e,0x3f80,0x3f623ef1,0x3f35bf35,0xbe95bf75,0x3f808000,0x3ef1bf62,0xbf35bf35,0xbf753e95,
0x3f80,0x3f6d3ec4,0xbf80,0xbf6dbec4,0x3f808000,0x3ec4bf6d,0xbf808000,0xbec43f6d,0x3f80,0x3f753e95,0xbf35bf35,0xbf623ef1,
0x3f808000,0x3e95bf75,0xbf353f35,0x3ef13f62,0x3f80,0x3f7b3e48,0xbf800000,0xbe483f7b,0x3f808000,0x3e48bf7b,0x3f80,0x3f7b3e48,
0x3f80,0x3f7f3dc9,0xbf353f35,0x3f223f46,0x3f808000,0x3dc9bf7f,0x3f353f35,0x3f46bf22,0x3f800000,0x3f35bf35,0x3f800000,0x3f35bf35,
0xbf80,0xbf35bf35,0xbf80,0xbf35bf35,0x3f7bbe48,0x3ef1bf62,0x3f0ebf55,0xbe95bf75,0xbe48bf7b,0xbf62bef1,0xbf55bf0e,0xbf753e95,
0x3f6dbec4,0x3e48bf7b,0xbec4bf6d,0xbf7bbe48,0xbec4bf6d,0xbf7bbe48,0xbf6d3ec4,0xbe483f7b,0x3f55bf0e,0xbdc9bf7f,0xbf7bbe48,0xbf223f46,
0xbf0ebf55,0xbf7f3dc9,0xbe483f7b,0x3f463f22,0x3f35bf35,0xbec4bf6d,0xbf353f35,0x3ec43f6d,0xbf35bf35,0xbf6d3ec4,0x3f353f35,0x3f6dbec4,
0x3f0ebf55,0xbf22bf46,0x3e483f7b,0x3f7f3dc9,0xbf55bf0e,0xbf463f22,0x3f7bbe48,0x3dc9bf7f,0x3ec4bf6d,0xbf55bf0e,0x3f6d3ec4,0x3f0ebf55,
0xbf6dbec4,0xbf0e3f55,0x3ec4bf6d,0xbf55bf0e,0x3e48bf7b,0xbf75be95,0x3f55bf0e,0xbef1bf62,0xbf7bbe48,0xbe953f75,0xbf0ebf55,0xbf623ef1,
0xbf80,0xbf35bf35,0xbf80,0xbf35bf35,0xbf808000,0xbf353f35,0xbf808000,0xbf353f35,0xbec4bf6d,0xbf75be95,0xbf6dbec4,0xbf623ef1,
0xbf6d3ec4,0xbe953f75,0xbec43f6d,0x3ef13f62,0xbf35bf35,0xbf7b3e48,0xbf353f35,0x3e483f7b,0xbf353f35,0x3e483f7b,0x3f353f35,0x3f7bbe48,
0xbf6dbec4,0xbf463f22,0x3ec43f6d,0x3f7f3dc9,0xbec43f6d,0x3f223f46,0x3f6dbec4,0x3dc9bf7f,0xbf800000,0xbec43f6d,0x3f800000,0x3ec4bf6d,
0x3f80,0x3f6d3ec4,0xbf80,0xbf6dbec4,0xbf6d3ec4,0x3dc93f7f,0x3ec4bf6d,0xbf46bf22,0x3ec43f6d,0x3f7fbdc9,0xbf6dbec4,0xbf223f46,
0xbf353f35,0x3f0e3f55,0xbf35bf35,0xbf553f0e,0x3f353f35,0x3f55bf0e,0xbf353f35,0x3f0e3f55,0xbec43f6d,0x3f623ef1,0xbf6d3ec4,0x3e953f75,
0x3f6d3ec4,0x3ef1bf62,0x3ec43f6d,0x3f75be95,0xbf800000,0xbf353f35,0xbf800000,0xbf353f35,0x3f80,0x3f353f35,0x3f80,0x3f353f35,
0xbf553f0e,0xbdc93f7f,0xbe483f7b,0x3f223f46,0x3f0e3f55,0x3f7f3dc9,0x3f7b3e48,0x3f46bf22,0xbec43f6d,0x3f0e3f55,0x3f6d3ec4,0x3f55bf0e,
0x3f6d3ec4,0x3f55bf0e,0x3ec4bf6d,0xbf0ebf55,0x3e483f7b,0x3f753e95,0x3f0ebf55,0xbef1bf62,0x3f7bbe48,0x3e95bf75,0xbf55bf0e,0xbf623ef1,
0x3f353f35,0x3f6dbec4,0xbf35bf35,0xbf6d3ec4,0x3f35bf35,0xbec4bf6d,0xbf353f35,0x3ec43f6d,0x3f7b3e48,0x3ef1bf62,0xbf553f0e,0x3e953f75,
0x3e48bf7b,0xbf62bef1,0x3f0e3f55,0x3f75be95,0x3f6dbec4,0xbe48bf7b,0x3ec43f6d,0x3f7bbe48,0xbec4bf6d,0xbf7b3e48,0x3f6dbec4,0xbe48bf7b,
0x3f0ebf55,0xbf46bf22,0x3f7bbe48,0xbdc9bf7f,0xbf55bf0e,0xbf223f46,0xbe48bf7b,0xbf7f3dc9,0x3f80,0x3f353f35,0x3f80,0x3f353f35,
0x3f808000,0x3f35bf35,0x3f808000,0x3f35bf35,0x3f800000,0x3f22bf46,0x3f35bf35,0xbdc9bf7f,0xbf80,0xbf46bf22,0xbf35bf35,0xbf7f3dc9,
0xbf80,0xbf55bf0e,0xbf800000,0xbf0e3f55,0xbf808000,0xbf0e3f55,0x3f80,0x3f553f0e,0xbf800000,0xbef13f62,0x3f353f35,0x3f75be95,
0x3f80,0x3f623ef1,0x3f35bf35,0xbe95bf75,0x3f80,0x3f6d3ec4,0xbf80,0xbf6dbec4,0x3f808000,0x3ec4bf6d,0xbf808000,0xbec43f6d,
0x3f800000,0x3e95bf75,0xbf353f35,0x3ef13f62,0xbf80,0xbf75be95,0x3f353f35,0x3f62bef1,0xbf80,0xbf7bbe48,0x3f800000,0x3e48bf7b,
0xbf808000,0xbe483f7b,0xbf80,0xbf7bbe48,0xbf800000,0xbdc93f7f,0xbf35bf35,0xbf463f22,0x3f80,0x3f7f3dc9,0xbf353f35,0x3f223f46,
0x3f800000,0x3f35bf35,0x3f800000,0x3f35bf35,0xbf80,0xbf35bf35,0xbf80,0xbf35bf35,0xbe48bf7b,0xbf62bef1,0xbf55bf0e,0xbf753e95,
0xbf7b3e48,0xbef13f62,0xbf0e3f55,0x3e953f75,0xbf6d3ec4,0xbe483f7b,0x3ec43f6d,0x3f7b3e48,0x3ec43f6d,0x3f7b3e48,0x3f6dbec4,0x3e48bf7b,
0x3f0e3f55,0x3f7fbdc9,0x3e48bf7b,0xbf46bf22,0x3f55bf0e,0xbdc9bf7f,0xbf7bbe48,0xbf223f46,0x3f35bf35,0xbec4bf6d,0xbf353f35,0x3ec43f6d,
0xbf35bf35,0xbf6d3ec4,0x3f353f35,0x3f6dbec4,0xbf55bf0e,0xbf463f22,0x3f7bbe48,0x3dc9bf7f,0xbf0e3f55,0x3f223f46,0xbe48bf7b,0xbf7fbdc9,
0xbec43f6d,0x3f553f0e,0xbf6dbec4,0xbf0e3f55,0x3f6d3ec4,0x3f0ebf55,0xbec43f6d,0x3f553f0e,0x3f7b3e48,0x3e95bf75,0x3f0e3f55,0x3f62bef1,
0x3e48bf7b,0xbf75be95,0x3f55bf0e,0xbef1bf62,0xbf80,0xbf35bf35,0xbf80,0xbf35bf35,0xbf808000,0xbf353f35,0xbf808000,0xbf353f35,
0xbf6d3ec4,0xbe953f75,0xbec43f6d,0x3ef13f62,0x3ec43f6d,0x3f753e95,0x3f6d3ec4,0x3f62bef1,0x3f353f35,0x3f7bbe48,0x3f35bf35,0xbe48bf7b,
0x3f35bf35,0xbe48bf7b,0xbf35bf35,0xbf7b3e48,0x3ec4bf6d,0xbf22bf46,0xbf6d3ec4,0xbdc93f7f,0xbf6dbec4,0xbf463f22,0x3ec43f6d,0x3f7f3dc9,
0xbf800000,0xbec43f6d,0x3f800000,0x3ec4bf6d,0x3f80,0x3f6d3ec4,0xbf80,0xbf6dbec4,0x3ec43f6d,0x3f7fbdc9,0xbf6dbec4,0xbf223f46,
0x3f6dbec4,0xbdc9bf7f,0xbec43f6d,0x3f463f22,0x3f35bf35,0xbf0ebf55,0x3f353f35,0x3f55bf0e,0xbf35bf35,0xbf553f0e,0x3f35bf35,0xbf0ebf55,
0xbf6dbec4,0xbef13f62,0xbec4bf6d,0xbf753e95,0xbec43f6d,0x3f623ef1,0xbf6d3ec4,0x3e953f75,0xbf800000,0xbf353f35,0xbf800000,0xbf353f35,
0x3f80,0x3f353f35,0x3f80,0x3f353f35,0x3f0e3f55,0x3f7f3dc9,0x3f7b3e48,0x3f46bf22,0x3f55bf0e,0x3dc9bf7f,0x3e48bf7b,0xbf22bf46,
0x3ec4bf6d,0xbf0ebf55,0xbf6dbec4,0xbf553f0e,0xbf6dbec4,0xbf553f0e,0xbec43f6d,0x3f0e3f55,0xbf7b3e48,0xbe953f75,0x3f553f0e,0x3f62bef1,
0x3e483f7b,0x3f753e95,0x3f0ebf55,0xbef1bf62,0x3f353f35,0x3f6dbec4,0xbf35bf35,0xbf6d3ec4,0x3f35bf35,0xbec4bf6d,0xbf353f35,0x3ec43f6d,
0x3e48bf7b,0xbf62bef1,0x3f0e3f55,0x3f75be95,0xbf7bbe48,0xbef13f62,0x3f55bf0e,0xbe95bf75,0xbf6d3ec4,0x3e483f7b,0xbec4bf6d,0xbf7b3e48,
0x3ec43f6d,0x3f7bbe48,0xbf6d3ec4,0x3e483f7b,0x3f553f0e,0x3f22bf46,0x3e483f7b,0x3f7fbdc9,0x3f0ebf55,0xbf46bf22,0x3f7bbe48,0xbdc9bf7f,
0x3f80,0x3f353f35,0x3f80,0x3f353f35,0x3f808000,0x3f35bf35,0x3f808000,0x3f35bf35,0xbf80,0xbf46bf22,0xbf35bf35,0xbf7f3dc9,
0xbf808000,0xbf223f46,0xbf353f35,0x3dc93f7f,0x3f80,0x3f553f0e,0x3f800000,0x3f0ebf55,0x3f808000,0x3f0ebf55,0xbf80,0xbf55bf0e,
0xbf80,0xbf62bef1,0xbf353f35,0x3e953f75,0xbf808000,0xbef13f62,0x3f353f35,0x3f75be95,0x3f80,0x3f6d3ec4,0xbf80,0xbf6dbec4,
0x3f808000,0x3ec4bf6d,0xbf808000,0xbec43f6d,0xbf80,0xbf75be95,0x3f353f35,0x3f62bef1,0xbf808000,0xbe953f75,0x3f35bf35,0xbef1bf62,
0x3f80,0x3f7b3e48,0xbf800000,0xbe483f7b,0x3f808000,0x3e48bf7b,0x3f80,0x3f7b3e48,0xbf80,0xbf7fbdc9,0x3f35bf35,0xbf22bf46,
0xbf808000,0xbdc93f7f,0xbf35bf35,0xbf463f22,0x3f800000,0x3f35bf35,0x3f800000,0x3f35bf35,0xbf80,0xbf35bf35,0xbf80,0xbf35bf35,
0xbf7b3e48,0xbef13f62,0xbf0e3f55,0x3e953f75,0x3e483f7b,0x3f623ef1,0x3f553f0e,0x3f75be95,0x3f6dbec4,0x3e48bf7b,0xbec4bf6d,0xbf7bbe48,
0xbec4bf6d,0xbf7bbe48,0xbf6d3ec4,0xbe483f7b,0xbf553f0e,0x3dc93f7f,0x3f7b3e48,0x3f22bf46,0x3f0e3f55,0x3f7fbdc9,0x3e48bf7b,0xbf46bf22,
0x3f35bf35,0xbec4bf6d,0xbf353f35,0x3ec43f6d,0xbf35bf35,0xbf6d3ec4,0x3f353f35,0x3f6dbec4,0xbf0e3f55,0x3f223f46,0xbe48bf7b,0xbf7fbdc9,
0x3f553f0e,0x3f46bf22,0xbf7b3e48,0xbdc93f7f,0x3ec4bf6d,0xbf55bf0e,0x3f6d3ec4,0x3f0ebf55,0xbf6dbec4,0xbf0e3f55,0x3ec4bf6d,0xbf55bf0e,
0xbe483f7b,0x3f753e95,0xbf553f0e,0x3ef13f62,0x3f7b3e48,0x3e95bf75,0x3f0e3f55,0x3f62bef1,0xbf80,0xbf35bf35,0xbf80,0xbf35bf35,
0xbf808000,0xbf353f35,0xbf808000,0xbf353f35,0x3ec43f6d,0x3f753e95,0x3f6d3ec4,0x3f62bef1,0x3f6dbec4,0x3e95bf75,0x3ec4bf6d,0xbef1bf62,
0xbf35bf35,0xbf7b3e48,0xbf353f35,0x3e483f7b,0xbf353f35,0x3e483f7b,0x3f353f35,0x3f7bbe48,0x3f6d3ec4,0x3f46bf22,0xbec4bf6d,0xbf7fbdc9,
0x3ec4bf6d,0xbf22bf46,0xbf6d3ec4,0xbdc93f7f,0xbf800000,0xbec43f6d,0x3f800000,0x3ec4bf6d,0x3f80,0x3f6d3ec4,0xbf80,0xbf6dbec4,
0x3f6dbec4,0xbdc9bf7f,0xbec43f6d,0x3f463f22,0xbec4bf6d,0xbf7f3dc9,0x3f6d3ec4,0x3f22bf46,0xbf353f35,0x3f0e3f55,0xbf35bf35,0xbf553f0e,
0x3f353f35,0x3f55bf0e,0xbf353f35,0x3f0e3f55,0x3ec4bf6d,0xbf62bef1,0x3f6dbec4,0xbe95bf75,0xbf6dbec4,0xbef13f62,0xbec4bf6d,0xbf753e95,
0xbf800000,0xbf353f35,0xbf800000,0xbf353f35,0x3f80,0x3f353f35,0x3f80,0x3f353f35,0x3f55bf0e,0x3dc9bf7f,0x3e48bf7b,0xbf22bf46,
0xbf0ebf55,0xbf7fbdc9,0xbf7bbe48,0xbf463f22,0xbec43f6d,0x3f0e3f55,0x3f6d3ec4,0x3f55bf0e,0x3f6d3ec4,0x3f55bf0e,0x3ec4bf6d,0xbf0ebf55,
0xbe48bf7b,0xbf75be95,0xbf0e3f55,0x3ef13f62,0xbf7b3e48,0xbe953f75,0x3f553f0e,0x3f62bef1,0x3f353f35,0x3f6dbec4,0xbf35bf35,0xbf6d3ec4,
0x3f35bf35,0xbec4bf6d,0xbf353f35,0x3ec43f6d,0xbf7bbe48,0xbef13f62,0x3f55bf0e,0xbe95bf75,0xbe483f7b,0x3f623ef1,0xbf0ebf55,0xbf753e95,
0x3f6dbec4,0xbe48bf7b,0x3ec43f6d,0x3f7bbe48,0xbec4bf6d,0xbf7b3e48,0x3f6dbec4,0xbe48bf7b,0xbf0e3f55,0x3f463f22,0xbf7b3e48,0x3dc93f7f,
0x3f553f0e,0x3f22bf46,0x3e483f7b,0x3f7fbdc9,0x3f80,0x3f353f35,0x3f80,0x3f353f35,0x3f808000,0x3f35bf35,0x3f808000,0x3f35bf35,
0xbf800000,0xbf223f46,0xbf353f35,0x3dc93f7f,0x3f80,0x3f463f22,0x3f353f35,0x3f7fbdc9,0xbf80,0xbf55bf0e,0xbf800000,0xbf0e3f55,
0xbf808000,0xbf0e3f55,0x3f80,0x3f553f0e,0x3f800000,0x3ef1bf62,0xbf35bf35,0xbf753e95,0xbf80,0xbf62bef1,0xbf353f35,0x3e953f75,
0x3f80,0x3f6d3ec4,0xbf80,0xbf6dbec4,0x3f808000,0x3ec4bf6d,0xbf808000,0xbec43f6d,0xbf800000,0xbe953f75,0x3f35bf35,0xbef1bf62,
0x3f80,0x3f753e95,0xbf35bf35,0xbf623ef1,0xbf80,0xbf7bbe48,0x3f800000,0x3e48bf7b,0xbf808000,0xbe483f7b,0xbf80,0xbf7bbe48,
0x3f800000,0x3dc9bf7f,0x3f353f35,0x3f46bf22,0xbf80,0xbf7fbdc9,0x3f35bf35,0xbf22bf46,0x3f800000,0x3f35bf35,0x3f800000,0x3f35bf35,
0xbf80,0xbf35bf35,0xbf80,0xbf35bf35,0x3e483f7b,0x3f623ef1,0x3f553f0e,0x3f75be95,0x3f7bbe48,0x3ef1bf62,0x3f0ebf55,0xbe95bf75,
0xbf6d3ec4,0xbe483f7b,0x3ec43f6d,0x3f7b3e48,0x3ec43f6d,0x3f7b3e48,0x3f6dbec4,0x3e48bf7b,0xbf0ebf55,0xbf7f3dc9,0xbe483f7b,0x3f463f22,
0xbf553f0e,0x3dc93f7f,0x3f7b3e48,0x3f22bf46,0x3f35bf35,0xbec4bf6d,0xbf353f35,0x3ec43f6d,0xbf35bf35,0xbf6d3ec4,0x3f353f35,0x3f6dbec4,
0x3f553f0e,0x3f46bf22,0xbf7b3e48,0xbdc93f7f,0x3f0ebf55,0xbf22bf46,0x3e483f7b,0x3f7f3dc9,0xbec43f6d,0x3f553f0e,0xbf6dbec4,0xbf0e3f55,
0x3f6d3ec4,0x3f0ebf55,0xbec43f6d,0x3f553f0e,0xbf7bbe48,0xbe953f75,0xbf0ebf55,0xbf623ef1,0xbe483f7b,0x3f753e95,0xbf553f0e,0x3ef13f62,
0xbf80,0xbf35bf35,0xbf80,0xbf35bf35,0xbf808000,0xbf353f35,0xbf808000,0xbf353f35,0x3f6dbec4,0x3e95bf75,0x3ec4bf6d,0xbef1bf62,
0xbec4bf6d,0xbf75be95,0xbf6dbec4,0xbf623ef1,0x3f353f35,0x3f7bbe48,0x3f35bf35,0xbe48bf7b,0x3f35bf35,0xbe48bf7b,0xbf35bf35,0xbf7b3e48,
0xbec43f6d,0x3f223f46,0x3f6dbec4,0x3dc9bf7f,0x3f6d3ec4,0x3f46bf22,0xbec4bf6d,0xbf7fbdc9,0xbf800000,0xbec43f6d,0x3f800000,0x3ec4bf6d,
0x3f80,0x3f6d3ec4,0xbf80,0xbf6dbec4,0xbec4bf6d,0xbf7f3dc9,0x3f6d3ec4,0x3f22bf46,0xbf6d3ec4,0x3dc93f7f,0x3ec4bf6d,0xbf46bf22,
0x3f35bf35,0xbf0ebf55,0x3f353f35,0x3f55bf0e,0xbf35bf35,0xbf553f0e,0x3f35bf35,0xbf0ebf55,0x3f6d3ec4,0x3ef1bf62,0x3ec43f6d,0x3f75be95,
0x3ec4bf6d,0xbf62bef1,0x3f6dbec4,0xbe95bf75,0xbf800000,0xbf353f35,0xbf800000,0xbf353f35,0x3f80,0x3f353f35,0x3f80,0x3f353f35,
0xbf0ebf55,0xbf7fbdc9,0xbf7bbe48,0xbf463f22,0xbf553f0e,0xbdc93f7f,0xbe483f7b,0x3f223f46,0x3ec4bf6d,0xbf0ebf55,0xbf6dbec4,0xbf553f0e,
0xbf6dbec4,0xbf553f0e,0xbec43f6d,0x3f0e3f55,0x3f7bbe48,0x3e95bf75,0xbf55bf0e,0xbf623ef1,0xbe48bf7b,0xbf75be95,0xbf0e3f55,0x3ef13f62,
0x3f353f35,0x3f6dbec4,0xbf35bf35,0xbf6d3ec4,0x3f35bf35,0xbec4bf6d,0xbf353f35,0x3ec43f6d,0xbe483f7b,0x3f623ef1,0xbf0ebf55,0xbf753e95,
0x3f7b3e48,0x3ef1bf62,0xbf553f0e,0x3e953f75,0xbf6d3ec4,0x3e483f7b,0xbec4bf6d,0xbf7b3e48,0x3ec43f6d,0x3f7bbe48,0xbf6d3ec4,0x3e483f7b,
0xbf55bf0e,0xbf223f46,0xbe48bf7b,0xbf7f3dc9,0xbf0e3f55,0x3f463f22,0xbf7b3e48,0x3dc93f7f,0x3f80,0x3f800000,0x3f80,0x3f800000,
0x3f808000,0xbf80,0x3f808000,0xbf80,0x3f80,0x3f7fbdc9,0x3f353f35,0x3f22bf46,0x3f808000,0xbdc9bf7f,0x3f35bf35,0xbf46bf22,
0x3f80,0x3f7bbe48,0x3f800000,0xbe48bf7b,0x3f808000,0xbe48bf7b,0xbf80,0xbf7b3e48,0x3f80,0x3f75be95,0x3f35bf35,0xbf62bef1,
0x3f808000,0xbe95bf75,0xbf35bf35,0xbef13f62,0x3f80,0x3f6dbec4,0xbf80,0xbf6d3ec4,0x3f808000,0xbec4bf6d,0xbf808000,0x3ec43f6d,
0x3f80,0x3f62bef1,0xbf35bf35,0xbe953f75,0x3f808000,0xbef1bf62,0xbf353f35,0x3f753e95,0x3f80,0x3f55bf0e,0xbf800000,0x3f0e3f55,
0x3f808000,0xbf0ebf55,0x3f80,0x3f55bf0e,0x3f80,0x3f46bf22,0xbf353f35,0x3f7f3dc9,0x3f808000,0xbf22bf46,0x3f353f35,0x3dc9bf7f,
0xbf80,0xbf800000,0xbf80,0xbf800000,0xbf808000,0x3f80,0xbf808000,0x3f80,0xbe48bf7b,0xbf753e95,0xbf55bf0e,0xbef13f62,
0xbf7b3e48,0x3e953f75,0xbf0e3f55,0x3f623ef1,0xbec4bf6d,0xbf553f0e,0xbf6d3ec4,0x3f0e3f55,0xbf6d3ec4,0x3f0e3f55,0x3ec43f6d,0x3f55bf0e,
0xbf0ebf55,0xbf223f46,0xbe483f7b,0x3f7fbdc9,0xbf553f0e,0x3f463f22,0x3f7b3e48,0xbdc9bf7f,0xbf35bf35,0xbec43f6d,0x3f353f35,0x3ec4bf6d,
0xbf353f35,0x3f6d3ec4,0x3f35bf35,0xbf6dbec4,0xbf55bf0e,0xbdc93f7f,0x3f7bbe48,0xbf22bf46,0xbf0e3f55,0x3f7f3dc9,0xbe48bf7b,0xbf463f22,
0xbf6dbec4,0x3e483f7b,0x3ec4bf6d,0xbf7b3e48,0xbec43f6d,0x3f7bbe48,0xbf6dbec4,0x3e483f7b,0xbf7bbe48,0x3ef13f62,0xbf0ebf55,0xbe953f75,
0xbe483f7b,0x3f62bef1,0xbf553f0e,0x3f753e95,0x3f80,0x3f800000,0x3f80,0x3f800000,0x3f808000,0xbf80,0x3f808000,0xbf80,
0x3ec43f6d,0x3f62bef1,0x3f6d3ec4,0x3e95bf75,0x3f6dbec4,0xbef1bf62,0x3ec4bf6d,0xbf75be95,0x3f353f35,0x3f0ebf55,0x3f35bf35,0xbf55bf0e,
0x3f35bf35,0xbf55bf0e,0xbf35bf35,0xbf0e3f55,0x3f6d3ec4,0x3dc9bf7f,0xbec4bf6d,0xbf463f22,0x3ec4bf6d,0xbf7fbdc9,0xbf6d3ec4,0x3f223f46,
0x3f800000,0xbec4bf6d,0xbf800000,0x3ec43f6d,0xbf80,0xbf6d3ec4,0x3f80,0x3f6dbec4,0x3f6dbec4,0xbf46bf22,0xbec43f6d,0x3f7fbdc9,
0xbec4bf6d,0xbf223f46,0x3f6d3ec4,0xbdc9bf7f,0x3f35bf35,0xbf7bbe48,0x3f353f35,0x3e48bf7b,0xbf35bf35,0xbe483f7b,0x3f35bf35,0xbf7bbe48,
0x3ec4bf6d,0xbf753e95,0x3f6dbec4,0xbf62bef1,0xbf6dbec4,0x3e953f75,0xbec4bf6d,0xbef13f62,0xbf80,0xbf800000,0xbf80,0xbf800000,
0xbf808000,0x3f80,0xbf808000,0x3f80,0xbf0ebf55,0xbf463f22,0xbf7bbe48,0xbdc93f7f,0xbf553f0e,0x3f223f46,0xbe483f7b,0x3f7f3dc9,
0xbf6dbec4,0xbe483f7b,0xbec43f6d,0x3f7b3e48,0xbec43f6d,0x3f7b3e48,0x3f6d3ec4,0x3e48bf7b,0xbf7b3e48,0x3ef13f62,0x3f553f0e,0x3e95bf75,
0x3e483f7b,0x3f62bef1,0x3f0ebf55,0xbf75be95,0xbf353f35,0x3f6d3ec4,0x3f35bf35,0xbf6dbec4,0x3f353f35,0x3ec4bf6d,0xbf35bf35,0xbec43f6d,
0xbe483f7b,0x3f75be95,0xbf0ebf55,0xbef13f62,0x3f7b3e48,0xbe95bf75,0xbf553f0e,0x3f623ef1,0x3ec43f6d,0x3f0ebf55,0xbf6d3ec4,0x3f553f0e,
0x3f6dbec4,0xbf55bf0e,0x3ec43f6d,0x3f0ebf55,0x3f553f0e,0xbdc9bf7f,0x3e483f7b,0x3f22bf46,0x3f0ebf55,0xbf7f3dc9,0x3f7bbe48,0xbf46bf22,
0x3f80,0x3f800000,0x3f80,0x3f800000,0x3f808000,0xbf80,0x3f808000,0xbf80,0x3f800000,0xbdc9bf7f,0x3f35bf35,0xbf46bf22,
0xbf80,0xbf7f3dc9,0xbf35bf35,0xbf223f46,0xbf80,0xbf7b3e48,0xbf800000,0x3e483f7b,0xbf808000,0x3e483f7b,0x3f80,0x3f7bbe48,
0xbf800000,0x3e953f75,0x3f353f35,0x3ef1bf62,0x3f80,0x3f75be95,0x3f35bf35,0xbf62bef1,0x3f80,0x3f6dbec4,0xbf80,0xbf6d3ec4,
0x3f808000,0xbec4bf6d,0xbf808000,0x3ec43f6d,0x3f800000,0xbef1bf62,0xbf353f35,0x3f753e95,0xbf80,0xbf623ef1,0x3f353f35,0x3e95bf75,
0xbf80,0xbf553f0e,0x3f800000,0xbf0ebf55,0xbf808000,0x3f0e3f55,0xbf80,0xbf553f0e,0xbf800000,0x3f223f46,0xbf35bf35,0xbdc93f7f,
0x3f80,0x3f46bf22,0xbf353f35,0x3f7f3dc9,0xbf80,0xbf800000,0xbf80,0xbf800000,0xbf808000,0x3f80,0xbf808000,0x3f80,
0xbf7b3e48,0x3e953f75,0xbf0e3f55,0x3f623ef1,0x3e483f7b,0x3f75be95,0x3f553f0e,0x3ef1bf62,0x3ec43f6d,0x3f55bf0e,0x3f6dbec4,0xbf0ebf55,
0x3f6dbec4,0xbf0ebf55,0xbec4bf6d,0xbf553f0e,0x3f55bf0e,0xbf46bf22,0xbf7bbe48,0x3dc93f7f,0xbf0ebf55,0xbf223f46,0xbe483f7b,0x3f7fbdc9,
0xbf35bf35,0xbec43f6d,0x3f353f35,0x3ec4bf6d,0xbf353f35,0x3f6d3ec4,0x3f35bf35,0xbf6dbec4,0xbf0e3f55,0x3f7f3dc9,0xbe48bf7b,0xbf463f22,
0x3f553f0e,0x3dc9bf7f,0xbf7b3e48,0x3f223f46,0x3f6d3ec4,0xbe48bf7b,0xbec43f6d,0x3f7bbe48,0x3ec4bf6d,0xbf7b3e48,0x3f6d3ec4,0xbe48bf7b,
0x3e48bf7b,0xbf623ef1,0x3f55bf0e,0xbf75be95,0xbf7bbe48,0x3ef13f62,0xbf0ebf55,0xbe953f75,0x3f80,0x3f800000,0x3f80,0x3f800000,
0x3f808000,0xbf80,0x3f808000,0xbf80,0x3f6dbec4,0xbef1bf62,0x3ec4bf6d,0xbf75be95,0xbec4bf6d,0xbf623ef1,0xbf6dbec4,0xbe953f75,
0xbf35bf35,0xbf0e3f55,0xbf353f35,0x3f553f0e,0xbf353f35,0x3f553f0e,0x3f353f35,0x3f0ebf55,0xbec43f6d,0x3f7f3dc9,0x3f6dbec4,0xbf22bf46,
0x3f6d3ec4,0x3dc9bf7f,0xbec4bf6d,0xbf463f22,0x3f800000,0xbec4bf6d,0xbf800000,0x3ec43f6d,0xbf80,0xbf6d3ec4,0x3f80,0x3f6dbec4,
0xbec4bf6d,0xbf223f46,0x3f6d3ec4,0xbdc9bf7f,0xbf6d3ec4,0x3f463f22,0x3ec4bf6d,0xbf7f3dc9,0xbf353f35,0x3f7b3e48,0xbf35bf35,0xbe483f7b,
0x3f353f35,0x3e48bf7b,0xbf353f35,0x3f7b3e48,0x3f6d3ec4,0xbe95bf75,0x3ec43f6d,0x3ef1bf62,0x3ec4bf6d,0xbf753e95,0x3f6dbec4,0xbf62bef1,
0xbf80,0xbf800000,0xbf80,0xbf800000,0xbf808000,0x3f80,0xbf808000,0x3f80,0xbf553f0e,0x3f223f46,0xbe483f7b,0x3f7f3dc9,
0x3f0e3f55,0x3f46bf22,0x3f7b3e48,0x3dc9bf7f,0x3f6d3ec4,0x3e48bf7b,0x3ec4bf6d,0xbf7bbe48,0x3ec4bf6d,0xbf7bbe48,0xbf6dbec4,0xbe483f7b,
0xbe48bf7b,0xbf623ef1,0xbf0e3f55,0x3f753e95,0xbf7b3e48,0x3ef13f62,0x3f553f0e,0x3e95bf75,0xbf353f35,0x3f6d3ec4,0x3f35bf35,0xbf6dbec4,
0x3f353f35,0x3ec4bf6d,0xbf35bf35,0xbec43f6d,0x3f7b3e48,0xbe95bf75,0xbf553f0e,0x3f623ef1,0x3e48bf7b,0xbf753e95,0x3f0e3f55,0x3ef1bf62,
0xbec4bf6d,0xbf0e3f55,0x3f6dbec4,0xbf55bf0e,0xbf6d3ec4,0x3f553f0e,0xbec4bf6d,0xbf0e3f55,0xbf0e3f55,0x3f7fbdc9,0xbf7b3e48,0x3f463f22,
0x3f553f0e,0xbdc9bf7f,0x3e483f7b,0x3f22bf46,0x3f80,0x3f800000,0x3f80,0x3f800000,0x3f808000,0xbf80,0x3f808000,0xbf80,
0xbf80,0xbf7f3dc9,0xbf35bf35,0xbf223f46,0xbf808000,0x3dc93f7f,0xbf353f35,0x3f463f22,0x3f80,0x3f7bbe48,0x3f800000,0xbe48bf7b,
0x3f808000,0xbe48bf7b,0xbf80,0xbf7b3e48,0xbf80,0xbf753e95,0xbf353f35,0x3f623ef1,0xbf808000,0x3e953f75,0x3f353f35,0x3ef1bf62,
0x3f80,0x3f6dbec4,0xbf80,0xbf6d3ec4,0x3f808000,0xbec4bf6d,0xbf808000,0x3ec43f6d,0xbf80,0xbf623ef1,0x3f353f35,0x3e95bf75,
0xbf808000,0x3ef13f62,0x3f35bf35,0xbf75be95,0x3f80,0x3f55bf0e,0xbf800000,0x3f0e3f55,0x3f808000,0xbf0ebf55,0x3f80,0x3f55bf0e,
0xbf80,0xbf463f22,0x3f35bf35,0xbf7fbdc9,0xbf808000,0x3f223f46,0xbf35bf35,0xbdc93f7f,0xbf80,0xbf800000,0xbf80,0xbf800000,
0xbf808000,0x3f80,0xbf808000,0x3f80,0x3e483f7b,0x3f75be95,0x3f553f0e,0x3ef1bf62,0x3f7bbe48,0xbe95bf75,0x3f0ebf55,0xbf62bef1,
0xbec4bf6d,0xbf553f0e,0xbf6d3ec4,0x3f0e3f55,0xbf6d3ec4,0x3f0e3f55,0x3ec43f6d,0x3f55bf0e,0x3f0e3f55,0x3f22bf46,0x3e48bf7b,0xbf7f3dc9,
0x3f55bf0e,0xbf46bf22,0xbf7bbe48,0x3dc93f7f,0xbf35bf35,0xbec43f6d,0x3f353f35,0x3ec4bf6d,0xbf353f35,0x3f6d3ec4,0x3f35bf35,0xbf6dbec4,
0x3f553f0e,0x3dc9bf7f,0xbf7b3e48,0x3f223f46,0x3f0ebf55,0xbf7fbdc9,0x3e483f7b,0x3f46bf22,0xbf6dbec4,0x3e483f7b,0x3ec4bf6d,0xbf7b3e48,
0xbec43f6d,0x3f7bbe48,0xbf6dbec4,0x3e483f7b,0x3f7b3e48,0xbef1bf62,0x3f0e3f55,0x3e95bf75,0x3e48bf7b,0xbf623ef1,0x3f55bf0e,0xbf75be95,
0x3f80,0x3f800000,0x3f80,0x3f800000,0x3f808000,0xbf80,0x3f808000,0xbf80,0xbec4bf6d,0xbf623ef1,0xbf6dbec4,0xbe953f75,
0xbf6d3ec4,0x3ef13f62,0xbec43f6d,0x3f753e95,0x3f353f35,0x3f0ebf55,0x3f35bf35,0xbf55bf0e,0x3f35bf35,0xbf55bf0e,0xbf35bf35,0xbf0e3f55,
0xbf6dbec4,0xbdc93f7f,0x3ec43f6d,0x3f46bf22,0xbec43f6d,0x3f7f3dc9,0x3f6dbec4,0xbf22bf46,0x3f800000,0xbec4bf6d,0xbf800000,0x3ec43f6d,
0xbf80,0xbf6d3ec4,0x3f80,0x3f6dbec4,0xbf6d3ec4,0x3f463f22,0x3ec4bf6d,0xbf7f3dc9,0x3ec43f6d,0x3f22bf46,0xbf6dbec4,0x3dc93f7f,
0x3f35bf35,0xbf7bbe48,0x3f353f35,0x3e48bf7b,0xbf35bf35,0xbe483f7b,0x3f35bf35,0xbf7bbe48,0xbec43f6d,0x3f75be95,0xbf6d3ec4,0x3f623ef1,
0x3f6d3ec4,0xbe95bf75,0x3ec43f6d,0x3ef1bf62,0xbf80,0xbf800000,0xbf80,0xbf800000,0xbf808000,0x3f80,0xbf808000,0x3f80,
0x3f0e3f55,0x3f46bf22,0x3f7b3e48,0x3dc9bf7f,0x3f55bf0e,0xbf22bf46,0x3e48bf7b,0xbf7fbdc9,0xbf6dbec4,0xbe483f7b,0xbec43f6d,0x3f7b3e48,
0xbec43f6d,0x3f7b3e48,0x3f6d3ec4,0x3e48bf7b,0x3f7bbe48,0xbef1bf62,0xbf55bf0e,0xbe953f75,0xbe48bf7b,0xbf623ef1,0xbf0e3f55,0x3f753e95,
0xbf353f35,0x3f6d3ec4,0x3f35bf35,0xbf6dbec4,0x3f353f35,0x3ec4bf6d,0xbf35bf35,0xbec43f6d,0x3e48bf7b,0xbf753e95,0x3f0e3f55,0x3ef1bf62,
0xbf7bbe48,0x3e953f75,0x3f55bf0e,0xbf62bef1,0x3ec43f6d,0x3f0ebf55,0xbf6d3ec4,0x3f553f0e,0x3f6dbec4,0xbf55bf0e,0x3ec43f6d,0x3f0ebf55,
0xbf55bf0e,0x3dc93f7f,0xbe48bf7b,0xbf223f46,0xbf0e3f55,0x3f7fbdc9,0xbf7b3e48,0x3f463f22,0x3f80,0x3f800000,0x3f80,0x3f800000,
0x3f808000,0xbf80,0x3f808000,0xbf80,0xbf800000,0x3dc93f7f,0xbf353f35,0x3f463f22,0x3f80,0x3f7fbdc9,0x3f353f35,0x3f22bf46,
0xbf80,0xbf7b3e48,0xbf800000,0x3e483f7b,0xbf808000,0x3e483f7b,0x3f80,0x3f7bbe48,0x3f800000,0xbe95bf75,0xbf35bf35,0xbef13f62,
0xbf80,0xbf753e95,0xbf353f35,0x3f623ef1,0x3f80,0x3f6dbec4,0xbf80,0xbf6d3ec4,0x3f808000,0xbec4bf6d,0xbf808000,0x3ec43f6d,
0xbf800000,0x3ef13f62,0x3f35bf35,0xbf75be95,0x3f80,0x3f62bef1,0xbf35bf35,0xbe953f75,0xbf80,0xbf553f0e,0x3f800000,0xbf0ebf55,
0xbf808000,0x3f0e3f55,0xbf80,0xbf553f0e,0x3f800000,0xbf22bf46,0x3f353f35,0x3dc9bf7f,0xbf80,0xbf463f22,0x3f35bf35,0xbf7fbdc9,
0xbf80,0xbf800000,0xbf80,0xbf800000,0xbf808000,0x3f80,0xbf808000,0x3f80,0x3f7bbe48,0xbe95bf75,0x3f0ebf55,0xbf62bef1,
0xbe48bf7b,0xbf753e95,0xbf55bf0e,0xbef13f62,0x3ec43f6d,0x3f55bf0e,0x3f6dbec4,0xbf0ebf55,0x3f6dbec4,0xbf0ebf55,0xbec4bf6d,0xbf553f0e,
0xbf553f0e,0x3f463f22,0x3f7b3e48,0xbdc9bf7f,0x3f0e3f55,0x3f22bf46,0x3e48bf7b,0xbf7f3dc9,0xbf35bf35,0xbec43f6d,0x3f353f35,0x3ec4bf6d,
0xbf353f35,0x3f6d3ec4,0x3f35bf35,0xbf6dbec4,0x3f0ebf55,0xbf7fbdc9,0x3e483f7b,0x3f46bf22,0xbf55bf0e,0xbdc93f7f,0x3f7bbe48,0xbf22bf46,
0x3f6d3ec4,0xbe48bf7b,0xbec43f6d,0x3f7bbe48,0x3ec4bf6d,0xbf7b3e48,0x3f6d3ec4,0xbe48bf7b,0xbe483f7b,0x3f62bef1,0xbf553f0e,0x3f753e95,
0x3f7b3e48,0xbef1bf62,0x3f0e3f55,0x3e95bf75,0x3f80,0x3f800000,0x3f80,0x3f800000,0x3f808000,0xbf80,0x3f808000,0xbf80,
0xbf6d3ec4,0x3ef13f62,0xbec43f6d,0x3f753e95,0x3ec43f6d,0x3f62bef1,0x3f6d3ec4,0x3e95bf75,0xbf35bf35,0xbf0e3f55,0xbf353f35,0x3f553f0e,
0xbf353f35,0x3f553f0e,0x3f353f35,0x3f0ebf55,0x3ec4bf6d,0xbf7fbdc9,0xbf6d3ec4,0x3f223f46,0xbf6dbec4,0xbdc93f7f,0x3ec43f6d,0x3f46bf22,
0x3f800000,0xbec4bf6d,0xbf800000,0x3ec43f6d,0xbf80,0xbf6d3ec4,0x3f80,0x3f6dbec4,0x3ec43f6d,0x3f22bf46,0xbf6dbec4,0x3dc93f7f,
0x3f6dbec4,0xbf46bf22,0xbec43f6d,0x3f7fbdc9,0xbf353f35,0x3f7b3e48,0xbf35bf35,0xbe483f7b,0x3f353f35,0x3e48bf7b,0xbf353f35,0x3f7b3e48,
0xbf6dbec4,0x3e953f75,0xbec4bf6d,0xbef13f62,0xbec43f6d,0x3f75be95,0xbf6d3ec4,0x3f623ef1,0xbf80,0xbf800000,0xbf80,0xbf800000,
0xbf808000,0x3f80,0xbf808000,0x3f80,0x3f55bf0e,0xbf22bf46,0x3e48bf7b,0xbf7fbdc9,0xbf0ebf55,0xbf463f22,0xbf7bbe48,0xbdc93f7f,
0x3f6d3ec4,0x3e48bf7b,0x3ec4bf6d,0xbf7bbe48,0x3ec4bf6d,0xbf7bbe48,0xbf6dbec4,0xbe483f7b,0x3e483f7b,0x3f62bef1,0x3f0ebf55,0xbf75be95,
0x3f7bbe48,0xbef1bf62,0xbf55bf0e,0xbe953f75,0xbf353f35,0x3f6d3ec4,0x3f35bf35,0xbf6dbec4,0x3f353f35,0x3ec4bf6d,0xbf35bf35,0xbec43f6d,
0xbf7bbe48,0x3e953f75,0x3f55bf0e,0xbf62bef1,0xbe483f7b,0x3f75be95,0xbf0ebf55,0xbef13f62,0xbec4bf6d,0xbf0e3f55,0x3f6dbec4,0xbf55bf0e,
0xbf6d3ec4,0x3f553f0e,0xbec4bf6d,0xbf0e3f55,0x3f0ebf55,0xbf7f3dc9,0x3f7bbe48,0xbf46bf22,0xbf55bf0e,0x3dc93f7f,0xbe48bf7b,0xbf223f46,
0x3f80,0x3f35bf35,0x3f80,0x3f35bf35,0x3f808000,0xbf35bf35,0x3f808000,0xbf35bf35,0x3f80,0x3f22bf46,0x3f353f35,0xbdc9bf7f,
0x3f808000,0xbf46bf22,0x3f35bf35,0xbf7f3dc9,0x3f80,0x3f0ebf55,0x3f800000,0xbf55bf0e,0x3f808000,0xbf55bf0e,0xbf80,0xbf0e3f55,
0x3f80,0x3ef1bf62,0x3f35bf35,0xbf753e95,0x3f808000,0xbf62bef1,0xbf35bf35,0x3e953f75,0x3f80,0x3ec4bf6d,0xbf80,0xbec43f6d,
0x3f808000,0xbf6dbec4,0xbf808000,0x3f6d3ec4,0x3f80,0x3e95bf75,0xbf35bf35,0x3ef13f62,0x3f808000,0xbf75be95,0xbf353f35,0x3f62bef1,
0x3f80,0x3e48bf7b,0xbf800000,0x3f7b3e48,0x3f808000,0xbf7bbe48,0x3f80,0x3e48bf7b,0x3f80,0x3dc9bf7f,0xbf353f35,0x3f46bf22,
0x3f808000,0xbf7fbdc9,0x3f353f35,0xbf22bf46,0xbf800000,0x3f353f35,0xbf800000,0x3f353f35,0x3f80,0x3f35bf35,0x3f80,0x3f35bf35,
0xbf7b3e48,0x3f623ef1,0xbf0e3f55,0x3f75be95,0x3e483f7b,0x3ef1bf62,0x3f553f0e,0xbe95bf75,0xbf6d3ec4,0x3f7b3e48,0x3ec43f6d,0x3e48bf7b,
0x3ec43f6d,0x3e48bf7b,0x3f6dbec4,0xbf7bbe48,0xbf553f0e,0x3f7fbdc9,0x3f7b3e48,0xbf46bf22,0x3f0e3f55,0xbdc9bf7f,0x3e48bf7b,0xbf223f46,
0xbf353f35,0x3f6dbec4,0x3f35bf35,0xbf6d3ec4,0x3f353f35,0xbec4bf6d,0xbf35bf35,0x3ec43f6d,0xbf0e3f55,0x3f46bf22,0xbe48bf7b,0xbdc93f7f,
0x3f553f0e,0xbf22bf46,0xbf7b3e48,0x3f7f3dc9,0xbec43f6d,0x3f0ebf55,0xbf6dbec4,0x3f553f0e,0x3f6d3ec4,0xbf55bf0e,0xbec43f6d,0x3f0ebf55,
0xbe483f7b,0x3e95bf75,0xbf553f0e,0x3f62bef1,0x3f7b3e48,0xbf75be95,0x3f0e3f55,0xbef1bf62,0xbf80,0xbf353f35,0xbf80,0xbf353f35,
0xbf808000,0x3f353f35,0xbf808000,0x3f353f35,0xbec4bf6d,0xbe953f75,0xbf6dbec4,0x3ef13f62,0xbf6d3ec4,0x3f753e95,0xbec43f6d,0x3f62bef1,
0xbf35bf35,0x3e483f7b,0xbf353f35,0x3f7bbe48,0xbf353f35,0x3f7bbe48,0x3f353f35,0xbe48bf7b,0xbf6dbec4,0x3f223f46,0x3ec43f6d,0x3dc9bf7f,
0xbec43f6d,0x3f46bf22,0x3f6dbec4,0xbf7fbdc9,0xbf800000,0x3f6d3ec4,0x3f800000,0xbf6dbec4,0x3f80,0x3ec4bf6d,0xbf80,0xbec43f6d,
0xbf6d3ec4,0x3f7fbdc9,0x3ec4bf6d,0xbf223f46,0x3ec43f6d,0xbdc9bf7f,0xbf6dbec4,0x3f463f22,0xbf353f35,0x3f55bf0e,0xbf35bf35,0x3f0e3f55,
0x3f353f35,0xbf0ebf55,0xbf353f35,0x3f55bf0e,0xbec43f6d,0x3ef1bf62,0xbf6d3ec4,0x3f75be95,0x3f6d3ec4,0xbf62bef1,0x3ec43f6d,0xbe95bf75,
0x3f800000,0xbf35bf35,0x3f800000,0xbf35bf35,0xbf80,0xbf353f35,0xbf80,0xbf353f35,0x3f55bf0e,0xbf7fbdc9,0x3e48bf7b,0xbf463f22,
0xbf0ebf55,0xbdc93f7f,0xbf7bbe48,0x3f223f46,0x3ec4bf6d,0xbf553f0e,0xbf6dbec4,0x3f0e3f55,0xbf6dbec4,0x3f0e3f55,0xbec43f6d,0x3f55bf0e,
0xbe48bf7b,0xbe953f75,0xbf0e3f55,0x3f62bef1,0xbf7b3e48,0x3f753e95,0x3f553f0e,0xbef1bf62,0xbf35bf35,0x3ec43f6d,0x3f353f35,0xbec4bf6d,
0xbf353f35,0x3f6dbec4,0x3f35bf35,0xbf6d3ec4,0xbf7bbe48,0x3f623ef1,0x3f55bf0e,0xbf753e95,0xbe483f7b,0x3ef1bf62,0xbf0ebf55,0x3e953f75,
0xbf6d3ec4,0x3f7bbe48,0xbec4bf6d,0x3e483f7b,0x3ec43f6d,0xbe48bf7b,0xbf6d3ec4,0x3f7bbe48,0xbf0e3f55,0x3f22bf46,0xbf7b3e48,0x3f7fbdc9,
0x3f553f0e,0xbf46bf22,0x3e483f7b,0xbdc9bf7f,0x3f80,0x3f35bf35,0x3f80,0x3f35bf35,0x3f808000,0xbf35bf35,0x3f808000,0xbf35bf35,
0x3f800000,0xbf46bf22,0x3f35bf35,0xbf7f3dc9,0xbf80,0xbf223f46,0xbf35bf35,0x3dc93f7f,0xbf80,0xbf0e3f55,0xbf800000,0x3f553f0e,
0xbf808000,0x3f553f0e,0x3f80,0x3f0ebf55,0xbf800000,0x3f623ef1,0x3f353f35,0xbe95bf75,0x3f80,0x3ef1bf62,0x3f35bf35,0xbf753e95,
0x3f80,0x3ec4bf6d,0xbf80,0xbec43f6d,0x3f808000,0xbf6dbec4,0xbf808000,0x3f6d3ec4,0x3f800000,0xbf75be95,0xbf353f35,0x3f62bef1,
0xbf80,0xbe953f75,0x3f353f35,0xbef1bf62,0xbf80,0xbe483f7b,0x3f800000,0xbf7bbe48,0xbf808000,0x3f7b3e48,0xbf80,0xbe483f7b,
0xbf800000,0x3f7f3dc9,0xbf35bf35,0x3f223f46,0x3f80,0x3dc9bf7f,0xbf353f35,0x3f46bf22,0xbf800000,0x3f353f35,0xbf800000,0x3f353f35,
0x3f80,0x3f35bf35,0x3f80,0x3f35bf35,0x3e483f7b,0x3ef1bf62,0x3f553f0e,0xbe95bf75,0x3f7bbe48,0xbf62bef1,0x3f0ebf55,0xbf753e95,
0x3f6dbec4,0xbf7bbe48,0xbec4bf6d,0xbe483f7b,0xbec4bf6d,0xbe483f7b,0xbf6d3ec4,0x3f7b3e48,0xbf0ebf55,0x3dc93f7f,0xbe483f7b,0x3f22bf46,
0xbf553f0e,0x3f7fbdc9,0x3f7b3e48,0xbf46bf22,0xbf353f35,0x3f6dbec4,0x3f35bf35,0xbf6d3ec4,0x3f353f35,0xbec4bf6d,0xbf35bf35,0x3ec43f6d,
0x3f553f0e,0xbf22bf46,0xbf7b3e48,0x3f7f3dc9,0x3f0ebf55,0xbf463f22,0x3e483f7b,0x3dc9bf7f,0x3ec4bf6d,0xbf0e3f55,0x3f6d3ec4,0xbf55bf0e,
0xbf6dbec4,0x3f553f0e,0x3ec4bf6d,0xbf0e3f55,0xbf7bbe48,0x3f753e95,0xbf0ebf55,0x3ef13f62,0xbe483f7b,0x3e95bf75,0xbf553f0e,0x3f62bef1,
0xbf80,0xbf353f35,0xbf80,0xbf353f35,0xbf808000,0x3f353f35,0xbf808000,0x3f353f35,0xbf6d3ec4,0x3f753e95,0xbec43f6d,0x3f62bef1,
0x3ec43f6d,0x3e95bf75,0x3f6d3ec4,0xbef1bf62,0x3f353f35,0xbe48bf7b,0x3f35bf35,0xbf7b3e48,0x3f35bf35,0xbf7b3e48,0xbf35bf35,0x3e483f7b,
0x3ec4bf6d,0xbf463f22,0xbf6d3ec4,0x3f7f3dc9,0xbf6dbec4,0x3f223f46,0x3ec43f6d,0x3dc9bf7f,0xbf800000,0x3f6d3ec4,0x3f800000,0xbf6dbec4,
0x3f80,0x3ec4bf6d,0xbf80,0xbec43f6d,0x3ec43f6d,0xbdc9bf7f,0xbf6dbec4,0x3f463f22,0x3f6dbec4,0xbf7f3dc9,0xbec43f6d,0x3f22bf46,
0x3f35bf35,0xbf553f0e,0x3f353f35,0xbf0ebf55,0xbf35bf35,0x3f0e3f55,0x3f35bf35,0xbf553f0e,0xbf6dbec4,0x3f623ef1,0xbec4bf6d,0x3e953f75,
0xbec43f6d,0x3ef1bf62,0xbf6d3ec4,0x3f75be95,0x3f800000,0xbf35bf35,0x3f800000,0xbf35bf35,0xbf80,0xbf353f35,0xbf80,0xbf353f35,
0xbf0ebf55,0xbdc93f7f,0xbf7bbe48,0x3f223f46,0xbf553f0e,0x3f7f3dc9,0xbe483f7b,0x3f46bf22,0xbec43f6d,0x3f55bf0e,0x3f6d3ec4,0xbf0ebf55,
0x3f6d3ec4,0xbf0ebf55,0x3ec4bf6d,0xbf553f0e,0x3f7bbe48,0xbf75be95,0xbf55bf0e,0x3ef13f62,0xbe48bf7b,0xbe953f75,0xbf0e3f55,0x3f62bef1,
0xbf35bf35,0x3ec43f6d,0x3f353f35,0xbec4bf6d,0xbf353f35,0x3f6dbec4,0x3f35bf35,0xbf6d3ec4,0xbe483f7b,0x3ef1bf62,0xbf0ebf55,0x3e953f75,
0x3f7b3e48,0xbf62bef1,0xbf553f0e,0x3f75be95,0x3f6dbec4,0xbf7b3e48,0x3ec43f6d,0xbe48bf7b,0xbec4bf6d,0x3e483f7b,0x3f6dbec4,0xbf7b3e48,
0xbf55bf0e,0x3f463f22,0xbe48bf7b,0x3dc93f7f,0xbf0e3f55,0x3f22bf46,0xbf7b3e48,0x3f7fbdc9,0x3f80,0x3f35bf35,0x3f80,0x3f35bf35,
0x3f808000,0xbf35bf35,0x3f808000,0xbf35bf35,0xbf80,0xbf223f46,0xbf35bf35,0x3dc93f7f,0xbf808000,0x3f463f22,0xbf353f35,0x3f7fbdc9,
0x3f80,0x3f0ebf55,0x3f800000,0xbf55bf0e,0x3f808000,0xbf55bf0e,0xbf80,0xbf0e3f55,0xbf80,0xbef13f62,0xbf353f35,0x3f75be95,
0xbf808000,0x3f623ef1,0x3f353f35,0xbe95bf75,0x3f80,0x3ec4bf6d,0xbf80,0xbec43f6d,0x3f808000,0xbf6dbec4,0xbf808000,0x3f6d3ec4,
0xbf80,0xbe953f75,0x3f353f35,0xbef1bf62,0xbf808000,0x3f753e95,0x3f35bf35,0xbf623ef1,0x3f80,0x3e48bf7b,0xbf800000,0x3f7b3e48,
0x3f808000,0xbf7bbe48,0x3f80,0x3e48bf7b,0xbf80,0xbdc93f7f,0x3f35bf35,0xbf463f22,0xbf808000,0x3f7f3dc9,0xbf35bf35,0x3f223f46,
0xbf800000,0x3f353f35,0xbf800000,0x3f353f35,0x3f80,0x3f35bf35,0x3f80,0x3f35bf35,0x3f7bbe48,0xbf62bef1,0x3f0ebf55,0xbf753e95,
0xbe48bf7b,0xbef13f62,0xbf55bf0e,0x3e953f75,0xbf6d3ec4,0x3f7b3e48,0x3ec43f6d,0x3e48bf7b,0x3ec43f6d,0x3e48bf7b,0x3f6dbec4,0xbf7bbe48,
0x3f55bf0e,0xbf7f3dc9,0xbf7bbe48,0x3f463f22,0xbf0ebf55,0x3dc93f7f,0xbe483f7b,0x3f22bf46,0xbf353f35,0x3f6dbec4,0x3f35bf35,0xbf6d3ec4,
0x3f353f35,0xbec4bf6d,0xbf35bf35,0x3ec43f6d,0x3f0ebf55,0xbf463f22,0x3e483f7b,0x3dc9bf7f,0xbf55bf0e,0x3f223f46,0x3f7bbe48,0xbf7fbdc9,
0xbec43f6d,0x3f0ebf55,0xbf6dbec4,0x3f553f0e,0x3f6d3ec4,0xbf55bf0e,0xbec43f6d,0x3f0ebf55,0x3e48bf7b,0xbe953f75,0x3f55bf0e,0xbf623ef1,
0xbf7bbe48,0x3f753e95,0xbf0ebf55,0x3ef13f62,0xbf80,0xbf353f35,0xbf80,0xbf353f35,0xbf808000,0x3f353f35,0xbf808000,0x3f353f35,
0x3ec43f6d,0x3e95bf75,0x3f6d3ec4,0xbef1bf62,0x3f6dbec4,0xbf75be95,0x3ec4bf6d,0xbf623ef1,0xbf35bf35,0x3e483f7b,0xbf353f35,0x3f7bbe48,
0xbf353f35,0x3f7bbe48,0x3f353f35,0xbe48bf7b,0x3f6d3ec4,0xbf22bf46,0xbec4bf6d,0xbdc93f7f,0x3ec4bf6d,0xbf463f22,0xbf6d3ec4,0x3f7f3dc9,
0xbf800000,0x3f6d3ec4,0x3f800000,0xbf6dbec4,0x3f80,0x3ec4bf6d,0xbf80,0xbec43f6d,0x3f6dbec4,0xbf7f3dc9,0xbec43f6d,0x3f22bf46,
0xbec4bf6d,0x3dc93f7f,0x3f6d3ec4,0xbf46bf22,0xbf353f35,0x3f55bf0e,0xbf35bf35,0x3f0e3f55,0x3f353f35,0xbf0ebf55,0xbf353f35,0x3f55bf0e,
0x3ec4bf6d,0xbef13f62,0x3f6dbec4,0xbf753e95,0xbf6dbec4,0x3f623ef1,0xbec4bf6d,0x3e953f75,0x3f800000,0xbf35bf35,0x3f800000,0xbf35bf35,
0xbf80,0xbf353f35,0xbf80,0xbf353f35,0xbf553f0e,0x3f7f3dc9,0xbe483f7b,0x3f46bf22,0x3f0e3f55,0x3dc9bf7f,0x3f7b3e48,0xbf22bf46,
0x3ec4bf6d,0xbf553f0e,0xbf6dbec4,0x3f0e3f55,0xbf6dbec4,0x3f0e3f55,0xbec43f6d,0x3f55bf0e,0x3e483f7b,0x3e95bf75,0x3f0ebf55,0xbf623ef1,
0x3f7bbe48,0xbf75be95,0xbf55bf0e,0x3ef13f62,0xbf35bf35,0x3ec43f6d,0x3f353f35,0xbec4bf6d,0xbf353f35,0x3f6dbec4,0x3f35bf35,0xbf6d3ec4,
0x3f7b3e48,0xbf62bef1,0xbf553f0e,0x3f75be95,0x3e48bf7b,0xbef13f62,0x3f0e3f55,0xbe95bf75,0xbf6d3ec4,0x3f7bbe48,0xbec4bf6d,0x3e483f7b,
0x3ec43f6d,0xbe48bf7b,0xbf6d3ec4,0x3f7bbe48,0x3f0ebf55,0xbf223f46,0x3f7bbe48,0xbf7f3dc9,0xbf55bf0e,0x3f463f22,0xbe48bf7b,0x3dc93f7f,
0x3f80,0x3f35bf35,0x3f80,0x3f35bf35,0x3f808000,0xbf35bf35,0x3f808000,0xbf35bf35,0xbf800000,0x3f463f22,0xbf353f35,0x3f7fbdc9,
0x3f80,0x3f22bf46,0x3f353f35,0xbdc9bf7f,0xbf80,0xbf0e3f55,0xbf800000,0x3f553f0e,0xbf808000,0x3f553f0e,0x3f80,0x3f0ebf55,
0x3f800000,0xbf62bef1,0xbf35bf35,0x3e953f75,0xbf80,0xbef13f62,0xbf353f35,0x3f75be95,0x3f80,0x3ec4bf6d,0xbf80,0xbec43f6d,
0x3f808000,0xbf6dbec4,0xbf808000,0x3f6d3ec4,0xbf800000,0x3f753e95,0x3f35bf35,0xbf623ef1,0x3f80,0x3e95bf75,0xbf35bf35,0x3ef13f62,
0xbf80,0xbe483f7b,0x3f800000,0xbf7bbe48,0xbf808000,0x3f7b3e48,0xbf80,0xbe483f7b,0x3f800000,0xbf7fbdc9,0x3f353f35,0xbf22bf46,
0xbf80,0xbdc93f7f,0x3f35bf35,0xbf463f22,0xbf800000,0x3f353f35,0xbf800000,0x3f353f35,0x3f80,0x3f35bf35,0x3f80,0x3f35bf35,
0xbe48bf7b,0xbef13f62,0xbf55bf0e,0x3e953f75,0xbf7b3e48,0x3f623ef1,0xbf0e3f55,0x3f75be95,0x3f6dbec4,0xbf7bbe48,0xbec4bf6d,0xbe483f7b,
0xbec4bf6d,0xbe483f7b,0xbf6d3ec4,0x3f7b3e48,0x3f0e3f55,0xbdc9bf7f,0x3e48bf7b,0xbf223f46,0x3f55bf0e,0xbf7f3dc9,0xbf7bbe48,0x3f463f22,
0xbf353f35,0x3f6dbec4,0x3f35bf35,0xbf6d3ec4,0x3f353f35,0xbec4bf6d,0xbf35bf35,0x3ec43f6d,0xbf55bf0e,0x3f223f46,0x3f7bbe48,0xbf7fbdc9,
0xbf0e3f55,0x3f46bf22,0xbe48bf7b,0xbdc93f7f,0x3ec4bf6d,0xbf0e3f55,0x3f6d3ec4,0xbf55bf0e,0xbf6dbec4,0x3f553f0e,0x3ec4bf6d,0xbf0e3f55,
0x3f7b3e48,0xbf75be95,0x3f0e3f55,0xbef1bf62,0x3e48bf7b,0xbe953f75,0x3f55bf0e,0xbf623ef1,0xbf80,0xbf353f35,0xbf80,0xbf353f35,
0xbf808000,0x3f353f35,0xbf808000,0x3f353f35,0x3f6dbec4,0xbf75be95,0x3ec4bf6d,0xbf623ef1,0xbec4bf6d,0xbe953f75,0xbf6dbec4,0x3ef13f62,
0x3f353f35,0xbe48bf7b,0x3f35bf35,0xbf7b3e48,0x3f35bf35,0xbf7b3e48,0xbf35bf35,0x3e483f7b,0xbec43f6d,0x3f46bf22,0x3f6dbec4,0xbf7fbdc9,
0x3f6d3ec4,0xbf22bf46,0xbec4bf6d,0xbdc93f7f,0xbf800000,0x3f6d3ec4,0x3f800000,0xbf6dbec4,0x3f80,0x3ec4bf6d,0xbf80,0xbec43f6d,
0xbec4bf6d,0x3dc93f7f,0x3f6d3ec4,0xbf46bf22,0xbf6d3ec4,0x3f7fbdc9,0x3ec4bf6d,0xbf223f46,0x3f35bf35,0xbf553f0e,0x3f353f35,0xbf0ebf55,
0xbf35bf35,0x3f0e3f55,0x3f35bf35,0xbf553f0e,0x3f6d3ec4,0xbf62bef1,0x3ec43f6d,0xbe95bf75,0x3ec4bf6d,0xbef13f62,0x3f6dbec4,0xbf753e95,
0x3f800000,0xbf35bf35,0x3f800000,0xbf35bf35,0xbf80,0xbf353f35,0xbf80,0xbf353f35,0x3f0e3f55,0x3dc9bf7f,0x3f7b3e48,0xbf22bf46,
0x3f55bf0e,0xbf7fbdc9,0x3e48bf7b,0xbf463f22,0xbec43f6d,0x3f55bf0e,0x3f6d3ec4,0xbf0ebf55,0x3f6d3ec4,0xbf0ebf55,0x3ec4bf6d,0xbf553f0e,
0xbf7b3e48,0x3f753e95,0x3f553f0e,0xbef1bf62,0x3e483f7b,0x3e95bf75,0x3f0ebf55,0xbf623ef1,0xbf35bf35,0x3ec43f6d,0x3f353f35,0xbec4bf6d,
0xbf353f35,0x3f6dbec4,0x3f35bf35,0xbf6d3ec4,0x3e48bf7b,0xbef13f62,0x3f0e3f55,0xbe95bf75,0xbf7bbe48,0x3f623ef1,0x3f55bf0e,0xbf753e95,
0x3f6dbec4,0xbf7b3e48,0x3ec43f6d,0xbe48bf7b,0xbec4bf6d,0x3e483f7b,0x3f6dbec4,0xbf7b3e48,0x3f553f0e,0xbf46bf22,0x3e483f7b,0xbdc9bf7f,
0x3f0ebf55,0xbf223f46,0x3f7bbe48,0xbf7f3dc9,
};
__device__ const unsigned __attribute__((aligned(256))) TAB_P3[2048] = {
0x80003f80,0x80003f80,0x80003f80,0x80003f80,0x3f800000,0x3f800000,0x3f800000,0x3f800000,0x80003f80,0xbe483f7b,0xbec43f6d,0xbf0e3f55,
0x3f800000,0x3f7b3e48,0x3f6d3ec4,0x3f553f0e,0x80003f80,0xbec43f6d,0xbf353f35,0xbf6d3ec4,0x3f800000,0x3f6d3ec4,0x3f353f35,0x3ec43f6d,
0x80003f80,0xbf0e3f55,0xbf6d3ec4,0xbf7bbe48,0x3f800000,0x3f553f0e,0x3ec43f6d,0xbe483f7b,0x80003f80,0xbf353f35,0xbf800000,0xbf35bf35,
0x3f800000,0x3f353f35,0x3f80,0xbf353f35,0x80003f80,0xbf553f0e,0xbf6dbec4,0xbe48bf7b,0x3f800000,0x3f0e3f55,0xbec43f6d,0xbf7b3e48,
0x80003f80,0xbf6d3ec4,0xbf35bf35,0x3ec4bf6d,0x3f800000,0x3ec43f6d,0xbf353f35,0xbf6dbec4,0x80003f80,0xbf7b3e48,0xbec4bf6d,0x3f55bf0e,
0x3f800000,0x3e483f7b,0xbf6d3ec4,0xbf0ebf55,0x80003f80,0xbf800000,0x8000bf80,0x3f800000,0x3f800000,0x3f80,0xbf800000,0xbf80,
0x80003f80,0xbf7bbe48,0x3ec4bf6d,0x3f553f0e,0x3f800000,0xbe483f7b,0xbf6dbec4,0x3f0ebf55,0x80003f80,0xbf6dbec4,0x3f35bf35,0x3ec43f6d,
0x3f800000,0xbec43f6d,0xbf35bf35,0x3f6dbec4,0x80003f80,0xbf55bf0e,0x3f6dbec4,0xbe483f7b,0x3f800000,0xbf0e3f55,0xbec4bf6d,0x3f7b3e48,
0x80003f80,0xbf35bf35,0x3f800000,0xbf353f35,0x3f800000,0xbf353f35,0xbf80,0x3f353f35,0x80003f80,0xbf0ebf55,0x3f6d3ec4,0xbf7b3e48,
0x3f800000,0xbf553f0e,0x3ec4bf6d,0x3e483f7b,0x80003f80,0xbec4bf6d,0x3f353f35,0xbf6dbec4,0x3f800000,0xbf6d3ec4,0x3f35bf35,0xbec43f6d,
0x80003f80,0xbe48bf7b,0x3ec43f6d,0xbf0ebf55,0x3f800000,0xbf7b3e48,0x3f6dbec4,0xbf553f0e,0x80003f80,0x80003f80,0x80003f80,0x80003f80,
0x3f800000,0x3f800000,0x3f800000,0x3f800000,0xbf353f35,0xbf553f0e,0xbf6d3ec4,0xbf7b3e48,0x3f353f35,0x3f0e3f55,0x3ec43f6d,0x3e483f7b,
0xbf800000,0xbf6dbec4,0xbf35bf35,0xbec4bf6d,0x3f80,0xbec43f6d,0xbf353f35,0xbf6d3ec4,0xbf35bf35,0xbe48bf7b,0x3ec4bf6d,0x3f55bf0e,
0xbf353f35,0xbf7b3e48,0xbf6dbec4,0xbf0ebf55,0x8000bf80,0x3f35bf35,0x3f800000,0x3f353f35,0xbf800000,0xbf35bf35,0xbf80,0x3f35bf35,
0x3f35bf35,0x3f7b3e48,0x3ec43f6d,0xbf0e3f55,0xbf35bf35,0x3e48bf7b,0x3f6dbec4,0x3f553f0e,0x3f800000,0x3ec43f6d,0xbf353f35,0xbf6dbec4,
0xbf80,0x3f6dbec4,0x3f353f35,0xbec43f6d,0x3f353f35,0xbf0e3f55,0xbf6dbec4,0x3e48bf7b,0x3f35bf35,0x3f553f0e,0xbec43f6d,0xbf7bbe48,
0x80003f80,0xbf800000,0x8000bf80,0x3f800000,0x3f800000,0x3f80,0xbf800000,0xbf80,0xbf353f35,0xbf0ebf55,0x3f6dbec4,0x3e483f7b,
0x3f353f35,0xbf553f0e,0xbec4bf6d,0x3f7bbe48,0xbf800000,0x3ec4bf6d,0x3f353f35,0xbf6d3ec4,0x3f80,0xbf6dbec4,0x3f35bf35,0x3ec43f6d,
0xbf35bf35,0x3f7bbe48,0xbec43f6d,0xbf0ebf55,0xbf353f35,0xbe48bf7b,0x3f6d3ec4,0xbf553f0e,0x8000bf80,0x3f353f35,0xbf800000,0x3f35bf35,
0xbf800000,0x3f35bf35,0x3f80,0xbf35bf35,0x3f35bf35,0xbe483f7b,0xbec4bf6d,0x3f553f0e,0xbf35bf35,0x3f7b3e48,0xbf6d3ec4,0x3f0ebf55,
0x3f800000,0xbf6d3ec4,0x3f35bf35,0xbec43f6d,0xbf80,0x3ec43f6d,0xbf35bf35,0x3f6d3ec4,0x3f353f35,0xbf55bf0e,0x3f6d3ec4,0xbf7bbe48,
0x3f35bf35,0xbf0e3f55,0x3ec4bf6d,0xbe483f7b,0x80003f80,0x80003f80,0x80003f80,0x80003f80,0x3f800000,0x3f800000,0x3f800000,0x3f800000,
0xbf800000,0xbf7bbe48,0xbf6dbec4,0xbf55bf0e,0x3f80,0xbe483f7b,0xbec43f6d,0xbf0e3f55,0x8000bf80,0x3ec4bf6d,0x3f35bf35,0x3f6dbec4,
0xbf800000,0xbf6dbec4,0xbf35bf35,0xbec4bf6d,0x3f800000,0x3f553f0e,0x3ec43f6d,0xbe483f7b,0xbf80,0x3f0ebf55,0x3f6dbec4,0x3f7b3e48,
0x80003f80,0xbf353f35,0xbf800000,0xbf35bf35,0x3f800000,0x3f353f35,0x3f80,0xbf353f35,0xbf800000,0xbf0ebf55,0x3ec4bf6d,0x3f7bbe48,
0x3f80,0xbf553f0e,0xbf6dbec4,0xbe48bf7b,0x8000bf80,0x3f6dbec4,0x3f353f35,0xbec43f6d,0xbf800000,0xbec4bf6d,0x3f35bf35,0x3f6d3ec4,
0x3f800000,0x3e483f7b,0xbf6d3ec4,0xbf0ebf55,0xbf80,0x3f7bbe48,0x3ec43f6d,0xbf553f0e,0x80003f80,0xbf800000,0x8000bf80,0x3f800000,
0x3f800000,0x3f80,0xbf800000,0xbf80,0xbf800000,0x3e48bf7b,0x3f6d3ec4,0xbf0e3f55,0x3f80,0xbf7bbe48,0x3ec4bf6d,0x3f553f0e,
0x8000bf80,0x3f6d3ec4,0xbf353f35,0xbec4bf6d,0xbf800000,0x3ec4bf6d,0x3f353f35,0xbf6d3ec4,0x3f800000,0xbf0e3f55,0xbec4bf6d,0x3f7b3e48,
0xbf80,0x3f553f0e,0xbf6d3ec4,0x3e48bf7b,0x80003f80,0xbf35bf35,0x3f800000,0xbf353f35,0x3f800000,0xbf353f35,0xbf80,0x3f353f35,
0xbf800000,0x3f55bf0e,0xbec43f6d,0xbe48bf7b,0x3f80,0xbf0ebf55,0x3f6d3ec4,0xbf7b3e48,0x8000bf80,0x3ec43f6d,0xbf35bf35,0x3f6d3ec4,
0xbf800000,0x3f6dbec4,0xbf353f35,0x3ec4bf6d,0x3f800000,0xbf7b3e48,0x3f6dbec4,0xbf553f0e,0xbf80,0x3e483f7b,0xbec4bf6d,0x3f0e3f55,
0x80003f80,0x80003f80,0x80003f80,0x80003f80,0x3f800000,0x3f800000,0x3f800000,0x3f800000,0xbf35bf35,0xbf0ebf55,0xbec4bf6d,0xbe48bf7b,
0xbf353f35,0xbf553f0e,0xbf6d3ec4,0xbf7b3e48,0x3f800000,0x3f6d3ec4,0x3f353f35,0x3ec43f6d,0xbf80,0x3ec4bf6d,0x3f35bf35,0x3f6dbec4,
0xbf353f35,0xbf7b3e48,0xbf6dbec4,0xbf0ebf55,0x3f353f35,0x3e483f7b,0xbec43f6d,0xbf553f0e,0x8000bf80,0x3f35bf35,0x3f800000,0x3f353f35,
0xbf800000,0xbf35bf35,0xbf80,0x3f35bf35,0x3f353f35,0xbe483f7b,0xbf6d3ec4,0xbf55bf0e,0x3f35bf35,0x3f7b3e48,0x3ec43f6d,0xbf0e3f55,
0xbf800000,0xbec4bf6d,0x3f35bf35,0x3f6d3ec4,0x3f80,0xbf6d3ec4,0xbf35bf35,0x3ec4bf6d,0x3f35bf35,0x3f553f0e,0xbec43f6d,0xbf7bbe48,
0xbf35bf35,0x3f0ebf55,0x3f6d3ec4,0xbe483f7b,0x80003f80,0xbf800000,0x8000bf80,0x3f800000,0x3f800000,0x3f80,0xbf800000,0xbf80,
0xbf35bf35,0x3f55bf0e,0x3ec43f6d,0xbf7b3e48,0xbf353f35,0xbf0ebf55,0x3f6dbec4,0x3e483f7b,0x3f800000,0xbec43f6d,0xbf35bf35,0x3f6dbec4,
0xbf80,0x3f6d3ec4,0xbf353f35,0xbec4bf6d,0xbf353f35,0xbe48bf7b,0x3f6d3ec4,0xbf553f0e,0x3f353f35,0xbf7b3e48,0x3ec4bf6d,0x3f0e3f55,
0x8000bf80,0x3f353f35,0xbf800000,0x3f35bf35,0xbf800000,0x3f35bf35,0x3f80,0xbf35bf35,0x3f353f35,0xbf7bbe48,0x3f6dbec4,0xbf0e3f55,
0x3f35bf35,0xbe483f7b,0xbec4bf6d,0x3f553f0e,0xbf800000,0x3f6dbec4,0xbf353f35,0x3ec4bf6d,0x3f80,0xbec4bf6d,0x3f353f35,0xbf6dbec4,
0x3f35bf35,0xbf0e3f55,0x3ec4bf6d,0xbe483f7b,0xbf35bf35,0x3f553f0e,0xbf6dbec4,0x3f7b3e48,0x3f80,0x3f80,0x3f80,0x3f80,
0xbf800000,0xbf800000,0xbf800000,0xbf800000,0xbf80,0xbe48bf7b,0xbec4bf6d,0xbf0ebf55,0x3f800000,0x3f7bbe48,0x3f6dbec4,0x3f55bf0e,
0x3f80,0x3ec43f6d,0x3f353f35,0x3f6d3ec4,0xbf800000,0xbf6d3ec4,0xbf353f35,0xbec43f6d,0xbf80,0xbf0ebf55,0xbf6dbec4,0xbf7b3e48,
0x3f800000,0x3f55bf0e,0x3ec4bf6d,0xbe48bf7b,0x3f80,0x3f353f35,0x3f800000,0x3f35bf35,0xbf800000,0xbf353f35,0x80003f80,0x3f353f35,
0xbf80,0xbf55bf0e,0xbf6d3ec4,0xbe483f7b,0x3f800000,0x3f0ebf55,0xbec4bf6d,0xbf7bbe48,0x3f80,0x3f6d3ec4,0x3f35bf35,0xbec4bf6d,
0xbf800000,0xbec43f6d,0x3f353f35,0x3f6dbec4,0xbf80,0xbf7bbe48,0xbec43f6d,0x3f553f0e,0x3f800000,0x3e48bf7b,0xbf6dbec4,0xbf0e3f55,
0x3f80,0x3f800000,0xbf80,0xbf800000,0xbf800000,0x80003f80,0x3f800000,0x8000bf80,0xbf80,0xbf7b3e48,0x3ec43f6d,0x3f55bf0e,
0x3f800000,0xbe48bf7b,0xbf6d3ec4,0x3f0e3f55,0x3f80,0x3f6dbec4,0xbf35bf35,0xbec43f6d,0xbf800000,0x3ec43f6d,0x3f35bf35,0xbf6dbec4,
0xbf80,0xbf553f0e,0x3f6d3ec4,0xbe48bf7b,0x3f800000,0xbf0ebf55,0xbec43f6d,0x3f7bbe48,0x3f80,0x3f35bf35,0xbf800000,0x3f353f35,
0xbf800000,0x3f353f35,0x8000bf80,0xbf353f35,0xbf80,0xbf0e3f55,0x3f6dbec4,0xbf7bbe48,0x3f800000,0xbf55bf0e,0x3ec43f6d,0x3e48bf7b,
0x3f80,0x3ec4bf6d,0xbf353f35,0x3f6dbec4,0xbf800000,0x3f6d3ec4,0xbf35bf35,0x3ec43f6d,0xbf80,0xbe483f7b,0x3ec4bf6d,0xbf0e3f55,
0x3f800000,0xbf7bbe48,0x3f6d3ec4,0xbf55bf0e,0x3f80,0x3f80,0x3f80,0x3f80,0xbf800000,0xbf800000,0xbf800000,0xbf800000,
0xbf35bf35,0xbf55bf0e,0xbf6dbec4,0xbf7bbe48,0x3f35bf35,0x3f0ebf55,0x3ec4bf6d,0x3e48bf7b,0x3f800000,0x3f6dbec4,0x3f35bf35,0x3ec4bf6d,
0x80003f80,0x3ec43f6d,0x3f353f35,0x3f6d3ec4,0xbf353f35,0xbe483f7b,0x3ec43f6d,0x3f553f0e,0xbf35bf35,0xbf7bbe48,0xbf6d3ec4,0xbf0e3f55,
0xbf80,0xbf35bf35,0xbf800000,0xbf353f35,0x3f800000,0x3f35bf35,0x8000bf80,0xbf35bf35,0x3f353f35,0x3f7bbe48,0x3ec4bf6d,0xbf0ebf55,
0xbf353f35,0x3e483f7b,0x3f6d3ec4,0x3f55bf0e,0xbf800000,0xbec43f6d,0x3f353f35,0x3f6dbec4,0x8000bf80,0xbf6dbec4,0xbf353f35,0x3ec43f6d,
0x3f35bf35,0xbf0ebf55,0xbf6d3ec4,0x3e483f7b,0x3f353f35,0x3f55bf0e,0xbec4bf6d,0xbf7b3e48,0x3f80,0x3f800000,0xbf80,0xbf800000,
0xbf800000,0x80003f80,0x3f800000,0x8000bf80,0xbf35bf35,0xbf0e3f55,0x3f6d3ec4,0x3e48bf7b,0x3f35bf35,0xbf55bf0e,0xbec43f6d,0x3f7b3e48,
0x3f800000,0xbec4bf6d,0xbf353f35,0x3f6d3ec4,0x80003f80,0x3f6dbec4,0xbf35bf35,0xbec43f6d,0xbf353f35,0x3f7b3e48,0xbec4bf6d,0xbf0e3f55,
0xbf35bf35,0xbe483f7b,0x3f6dbec4,0xbf55bf0e,0xbf80,0xbf353f35,0x3f800000,0xbf35bf35,0x3f800000,0xbf35bf35,0x80003f80,0x3f35bf35,
0x3f353f35,0xbe48bf7b,0xbec43f6d,0x3f55bf0e,0xbf353f35,0x3f7bbe48,0xbf6dbec4,0x3f0e3f55,0xbf800000,0x3f6d3ec4,0xbf35bf35,0x3ec43f6d,
0x8000bf80,0xbec43f6d,0x3f35bf35,0xbf6d3ec4,0x3f35bf35,0xbf553f0e,0x3f6dbec4,0xbf7b3e48,0x3f353f35,0xbf0ebf55,0x3ec43f6d,0xbe48bf7b,
0x3f80,0x3f80,0x3f80,0x3f80,0xbf800000,0xbf800000,0xbf800000,0xbf800000,0xbf800000,0xbf7b3e48,0xbf6d3ec4,0xbf553f0e,
0x8000bf80,0xbe48bf7b,0xbec4bf6d,0xbf0ebf55,0xbf80,0xbec4bf6d,0xbf35bf35,0xbf6dbec4,0x3f800000,0x3f6dbec4,0x3f35bf35,0x3ec4bf6d,
0x3f800000,0x3f55bf0e,0x3ec4bf6d,0xbe48bf7b,0x80003f80,0x3f0e3f55,0x3f6d3ec4,0x3f7bbe48,0x3f80,0x3f353f35,0x3f800000,0x3f35bf35,
0xbf800000,0xbf353f35,0x80003f80,0x3f353f35,0xbf800000,0xbf0e3f55,0x3ec43f6d,0x3f7b3e48,0x8000bf80,0xbf55bf0e,0xbf6d3ec4,0xbe483f7b,
0xbf80,0xbf6dbec4,0xbf353f35,0x3ec43f6d,0x3f800000,0x3ec4bf6d,0xbf35bf35,0xbf6d3ec4,0x3f800000,0x3e48bf7b,0xbf6dbec4,0xbf0e3f55,
0x80003f80,0x3f7b3e48,0x3ec4bf6d,0xbf55bf0e,0x3f80,0x3f800000,0xbf80,0xbf800000,0xbf800000,0x80003f80,0x3f800000,0x8000bf80,
0xbf800000,0x3e483f7b,0x3f6dbec4,0xbf0ebf55,0x8000bf80,0xbf7b3e48,0x3ec43f6d,0x3f55bf0e,0xbf80,0xbf6d3ec4,0x3f353f35,0x3ec4bf6d,
0x3f800000,0xbec4bf6d,0xbf353f35,0x3f6d3ec4,0x3f800000,0xbf0ebf55,0xbec43f6d,0x3f7bbe48,0x80003f80,0x3f55bf0e,0xbf6dbec4,0x3e483f7b,
0x3f80,0x3f35bf35,0xbf800000,0x3f353f35,0xbf800000,0x3f353f35,0x8000bf80,0xbf353f35,0xbf800000,0x3f553f0e,0xbec4bf6d,0xbe483f7b,
0x8000bf80,0xbf0e3f55,0x3f6dbec4,0xbf7bbe48,0xbf80,0xbec43f6d,0x3f35bf35,0xbf6d3ec4,0x3f800000,0xbf6dbec4,0x3f353f35,0xbec4bf6d,
0x3f800000,0xbf7bbe48,0x3f6d3ec4,0xbf55bf0e,0x80003f80,0x3e48bf7b,0xbec43f6d,0x3f0ebf55,0x3f80,0x3f80,0x3f80,0x3f80,
0xbf800000,0xbf800000,0xbf800000,0xbf800000,0xbf353f35,0xbf0e3f55,0xbec43f6d,0xbe483f7b,0xbf35bf35,0xbf55bf0e,0xbf6dbec4,0xbf7bbe48,
0xbf800000,0xbf6d3ec4,0xbf353f35,0xbec43f6d,0x8000bf80,0xbec4bf6d,0xbf35bf35,0xbf6dbec4,0xbf35bf35,0xbf7bbe48,0xbf6d3ec4,0xbf0e3f55,
0x3f35bf35,0x3e48bf7b,0xbec4bf6d,0xbf55bf0e,0xbf80,0xbf35bf35,0xbf800000,0xbf353f35,0x3f800000,0x3f35bf35,0x8000bf80,0xbf35bf35,
0x3f35bf35,0xbe48bf7b,0xbf6dbec4,0xbf553f0e,0x3f353f35,0x3f7bbe48,0x3ec4bf6d,0xbf0ebf55,0x3f800000,0x3ec4bf6d,0xbf35bf35,0xbf6d3ec4,
0x80003f80,0x3f6d3ec4,0x3f35bf35,0xbec4bf6d,0x3f353f35,0x3f55bf0e,0xbec4bf6d,0xbf7b3e48,0xbf353f35,0x3f0e3f55,0x3f6dbec4,0xbe48bf7b,
0x3f80,0x3f800000,0xbf80,0xbf800000,0xbf800000,0x80003f80,0x3f800000,0x8000bf80,0xbf353f35,0x3f553f0e,0x3ec4bf6d,0xbf7bbe48,
0xbf35bf35,0xbf0e3f55,0x3f6d3ec4,0x3e48bf7b,0xbf800000,0x3ec43f6d,0x3f35bf35,0xbf6dbec4,0x8000bf80,0xbf6d3ec4,0x3f353f35,0x3ec4bf6d,
0xbf35bf35,0xbe483f7b,0x3f6dbec4,0xbf55bf0e,0x3f35bf35,0xbf7bbe48,0x3ec43f6d,0x3f0ebf55,0xbf80,0xbf353f35,0x3f800000,0xbf35bf35,
0x3f800000,0xbf35bf35,0x80003f80,0x3f35bf35,0x3f35bf35,0xbf7b3e48,0x3f6d3ec4,0xbf0ebf55,0x3f353f35,0xbe48bf7b,0xbec43f6d,0x3f55bf0e,
0x3f800000,0xbf6dbec4,0x3f353f35,0xbec4bf6d,0x80003f80,0x3ec4bf6d,0xbf353f35,0x3f6dbec4,0x3f353f35,0xbf0ebf55,0x3ec43f6d,0xbe48bf7b,
0xbf353f35,0x3f55bf0e,0xbf6d3ec4,0x3f7bbe48,0x80003f80,0x80004000,0x80004000,0x80004000,0x80003f80,0xbec83ffb,0xbf443fed,0xbf8e3fd5,
0x80003f80,0xbf443fed,0xbfb53fb5,0xbfed3f44,0x80003f80,0xbf8e3fd5,0xbfed3f44,0xbffbbec8,0x80003f80,0xbfb53fb5,0xc0000000,0xbfb5bfb5,
0x80003f80,0xbfd53f8e,0xbfedbf44,0xbec8bffb,0x80003f80,0xbfed3f44,0xbfb5bfb5,0x3f44bfed,0x80003f80,0xbffb3ec8,0xbf44bfed,0x3fd5bf8e,
0x80003f80,0xc0000000,0x8000c000,0x40000000,0x80003f80,0xbffbbec8,0x3f44bfed,0x3fd53f8e,0x80003f80,0xbfedbf44,0x3fb5bfb5,0x3f443fed,
0x80003f80,0xbfd5bf8e,0x3fedbf44,0xbec83ffb,0x80003f80,0xbfb5bfb5,0x40000000,0xbfb53fb5,0x80003f80,0xbf8ebfd5,0x3fed3f44,0xbffb3ec8,
0x80003f80,0xbf44bfed,0x3fb53fb5,0xbfedbf44,0x80003f80,0xbec8bffb,0x3f443fed,0xbf8ebfd5,0x80003f80,0x8000c000,0x80004000,0x8000c000,
0x80003f80,0x3ec8bffb,0xbf443fed,0x3f8ebfd5,0x80003f80,0x3f44bfed,0xbfb53fb5,0x3fedbf44,0x80003f80,0x3f8ebfd5,0xbfed3f44,0x3ffb3ec8,
0x80003f80,0x3fb5bfb5,0xc0000000,0x3fb53fb5,0x80003f80,0x3fd5bf8e,0xbfedbf44,0x3ec83ffb,0x80003f80,0x3fedbf44,0xbfb5bfb5,0xbf443fed,
0x80003f80,0x3ffbbec8,0xbf44bfed,0xbfd53f8e,0x80003f80,0x40000000,0x8000c000,0xc0000000,0x80003f80,0x3ffb3ec8,0x3f44bfed,0xbfd5bf8e,
0x80003f80,0x3fed3f44,0x3fb5bfb5,0xbf44bfed,0x80003f80,0x3fd53f8e,0x3fedbf44,0x3ec8bffb,0x80003f80,0x3fb53fb5,0x40000000,0x3fb5bfb5,
0x80003f80,0x3f8e3fd5,0x3fed3f44,0x3ffbbec8,0x80003f80,0x3f443fed,0x3fb53fb5,0x3fed3f44,0x80003f80,0x3ec83ffb,0x3f443fed,0x3f8e3fd5,
0x80004000,0x80004000,0x80004000,0x80004000,0xbfb53fb5,0xbfd53f8e,0xbfed3f44,0xbffb3ec8,0xc0000000,0xbfedbf44,0xbfb5bfb5,0xbf44bfed,
0xbfb5bfb5,0xbec8bffb,0x3f44bfed,0x3fd5bf8e,0x8000c000,0x3fb5bfb5,0x40000000,0x3fb53fb5,0x3fb5bfb5,0x3ffb3ec8,0x3f443fed,0xbf8e3fd5,
0x40000000,0x3f443fed,0xbfb53fb5,0xbfedbf44,0x3fb53fb5,0xbf8e3fd5,0xbfedbf44,0x3ec8bffb,0x80004000,0xc0000000,0x8000c000,0x40000000,
0xbfb53fb5,0xbf8ebfd5,0x3fedbf44,0x3ec83ffb,0xc0000000,0x3f44bfed,0x3fb53fb5,0xbfed3f44,0xbfb5bfb5,0x3ffbbec8,0xbf443fed,0xbf8ebfd5,
0x8000c000,0x3fb53fb5,0xc0000000,0x3fb5bfb5,0x3fb5bfb5,0xbec83ffb,0xbf44bfed,0x3fd53f8e,0x40000000,0xbfed3f44,0x3fb5bfb5,0xbf443fed,
0x3fb53fb5,0xbfd5bf8e,0x3fed3f44,0xbffbbec8,0x80004000,0x8000c000,0x80004000,0x8000c000,0xbfb53fb5,0x3fd5bf8e,0xbfed3f44,0x3ffbbec8,
0xc0000000,0x3fed3f44,0xbfb5bfb5,0x3f443fed,0xbfb5bfb5,0x3ec83ffb,0x3f44bfed,0xbfd53f8e,0x8000c000,0xbfb53fb5,0x40000000,0xbfb5bfb5,
0x3fb5bfb5,0xbffbbec8,0x3f443fed,0x3f8ebfd5,0x40000000,0xbf44bfed,0xbfb53fb5,0x3fed3f44,0x3fb53fb5,0x3f8ebfd5,0xbfedbf44,0xbec83ffb,
0x80004000,0x40000000,0x8000c000,0xc0000000,0xbfb53fb5,0x3f8e3fd5,0x3fedbf44,0xbec8bffb,0xc0000000,0xbf443fed,0x3fb53fb5,0x3fedbf44,
0xbfb5bfb5,0xbffb3ec8,0xbf443fed,0x3f8e3fd5,0x8000c000,0xbfb5bfb5,0xc0000000,0xbfb53fb5,0x3fb5bfb5,0x3ec8bffb,0xbf44bfed,0xbfd5bf8e,
0x40000000,0x3fedbf44,0x3fb5bfb5,0x3f44bfed,0x3fb53fb5,0x3fd53f8e,0x3fed3f44,0x3ffb3ec8,0x80004000,0x80004000,0x80004000,0x80004000,
0xc0000000,0xbffbbec8,0xbfedbf44,0xbfd5bf8e,0x8000c000,0x3f44bfed,0x3fb5bfb5,0x3fedbf44,0x40000000,0x3fd53f8e,0x3f443fed,0xbec83ffb,
0x80004000,0xbfb53fb5,0xc0000000,0xbfb5bfb5,0xc0000000,0xbf8ebfd5,0x3f44bfed,0x3ffbbec8,0x8000c000,0x3fedbf44,0x3fb53fb5,0xbf443fed,
0x40000000,0x3ec83ffb,0xbfed3f44,0xbf8ebfd5,0x80004000,0xc0000000,0x8000c000,0x40000000,0xc0000000,0x3ec8bffb,0x3fed3f44,0xbf8e3fd5,
0x8000c000,0x3fed3f44,0xbfb53fb5,0xbf44bfed,0x40000000,0xbf8e3fd5,0xbf44bfed,0x3ffb3ec8,0x80004000,0xbfb5bfb5,0x40000000,0xbfb53fb5,
0xc0000000,0x3fd5bf8e,0xbf443fed,0xbec8bffb,0x8000c000,0x3f443fed,0xbfb5bfb5,0x3fed3f44,0x40000000,0xbffb3ec8,0x3fedbf44,0xbfd53f8e,
0x80004000,0x8000c000,0x80004000,0x8000c000,0xc0000000,0x3ffb3ec8,0xbfedbf44,0x3fd53f8e,0x8000c000,0xbf443fed,0x3fb5bfb5,0xbfed3f44,
0x40000000,0xbfd5bf8e,0x3f443fed,0x3ec8bffb,0x80004000,0x3fb5bfb5,0xc0000000,0x3fb53fb5,0xc0000000,0x3f8e3fd5,0x3f44bfed,0xbffb3ec8,
0x8000c000,0xbfed3f44,0x3fb53fb5,0x3f44bfed,0x40000000,0xbec8bffb,0xbfed3f44,0x3f8e3fd5,0x80004000,0x40000000,0x8000c000,0xc0000000,
0xc0000000,0xbec83ffb,0x3fed3f44,0x3f8ebfd5,0x8000c000,0xbfedbf44,0xbfb53fb5,0x3f443fed,0x40000000,0x3f8ebfd5,0xbf44bfed,0xbffbbec8,
0x80004000,0x3fb53fb5,0x40000000,0x3fb5bfb5,0xc0000000,0xbfd53f8e,0xbf443fed,0x3ec83ffb,0x8000c000,0xbf44bfed,0xbfb5bfb5,0xbfedbf44,
0x40000000,0x3ffbbec8,0x3fedbf44,0x3fd5bf8e,0x80004000,0x80004000,0x80004000,0x80004000,0xbfb5bfb5,0xbf8ebfd5,0xbf44bfed,0xbec8bffb,
0x40000000,0x3fed3f44,0x3fb53fb5,0x3f443fed,0xbfb53fb5,0xbffb3ec8,0xbfedbf44,0xbf8ebfd5,0x8000c000,0x3fb5bfb5,0x40000000,0x3fb53fb5,
0x3fb53fb5,0xbec83ffb,0xbfed3f44,0xbfd5bf8e,0xc0000000,0xbf44bfed,0x3fb5bfb5,0x3fed3f44,0x3fb5bfb5,0x3fd53f8e,0xbf443fed,0xbffbbec8,
0x80004000,0xc0000000,0x8000c000,0x40000000,0xbfb5bfb5,0x3fd5bf8e,0x3f443fed,0xbffb3ec8,0x40000000,0xbf443fed,0xbfb5bfb5,0x3fedbf44,
0xbfb53fb5,0xbec8bffb,0x3fed3f44,0xbfd53f8e,0x8000c000,0x3fb53fb5,0xc0000000,0x3fb5bfb5,0x3fb53fb5,0xbffbbec8,0x3fedbf44,0xbf8e3fd5,
0xc0000000,0x3fedbf44,0xbfb53fb5,0x3f44bfed,0x3fb5bfb5,0xbf8e3fd5,0x3f44bfed,0xbec83ffb,0x80004000,0x8000c000,0x80004000,0x8000c000,
0xbfb5bfb5,0x3f8e3fd5,0xbf44bfed,0x3ec83ffb,0x40000000,0xbfedbf44,0x3fb53fb5,0xbf44bfed,0xbfb53fb5,0x3ffbbec8,0xbfedbf44,0x3f8e3fd5,
0x8000c000,0xbfb53fb5,0x40000000,0xbfb5bfb5,0x3fb53fb5,0x3ec8bffb,0xbfed3f44,0x3fd53f8e,0xc0000000,0x3f443fed,0x3fb5bfb5,0xbfedbf44,
0x3fb5bfb5,0xbfd5bf8e,0xbf443fed,0x3ffb3ec8,0x80004000,0x40000000,0x8000c000,0xc0000000,0xbfb5bfb5,0xbfd53f8e,0x3f443fed,0x3ffbbec8,
0x40000000,0x3f44bfed,0xbfb5bfb5,0xbfed3f44,0xbfb53fb5,0x3ec83ffb,0x3fed3f44,0x3fd5bf8e,0x8000c000,0xbfb5bfb5,0xc0000000,0xbfb53fb5,
0x3fb53fb5,0x3ffb3ec8,0x3fedbf44,0x3f8ebfd5,0xc0000000,0xbfed3f44,0xbfb53fb5,0xbf443fed,0x3fb5bfb5,0x3f8ebfd5,0x3f44bfed,0x3ec8bffb,
0x80003f80,0x80000000,0x80000000,0x80000000,0x8000bf80,0x8000,0x8000,0x8000,0x80003f80,0x80000000,0x80000000,0x80000000,
0x8000bf80,0x8000,0x8000,0x0,0x80003f80,0x80000000,0x80000000,0x80008000,0x8000bf80,0x8000,0x0,0x0,
0x80003f80,0x80000000,0x80008000,0x8000,0x8000bf80,0x8000,0x0,0x80000000,0x80003f80,0x80000000,0x80008000,0x0,
0x8000bf80,0x0,0x80000000,0x80008000,0x80003f80,0x80008000,0x8000,0x0,0x8000bf80,0x0,0x80000000,0x8000,
0x80003f80,0x80008000,0x0,0x80000000,0x8000bf80,0x0,0x80008000,0x8000,0x80003f80,0x80008000,0x0,0x80008000,
0x8000bf80,0x0,0x80008000,0x0,0x80003f80,0x80008000,0x80000000,0x80008000,0x8000bf80,0x80000000,0x8000,0x80000000,
0x80003f80,0x8000,0x80000000,0x8000,0x8000bf80,0x80000000,0x8000,0x80008000,0x80003f80,0x8000,0x80000000,0x0,
0x8000bf80,0x80000000,0x0,0x80008000,0x80003f80,0x8000,0x80008000,0x80000000,0x8000bf80,0x80000000,0x0,0x8000,
0x80003f80,0x0,0x80008000,0x80000000,0x8000bf80,0x80008000,0x80000000,0x0,0x80003f80,0x0,0x8000,0x80008000,
0x8000bf80,0x80008000,0x80000000,0x80000000,0x80003f80,0x0,0x0,0x8000,0x8000bf80,0x80008000,0x80008000,0x80000000,
0x80003f80,0x0,0x0,0x0,0x8000bf80,0x80008000,0x80008000,0x80008000,0x80000000,0x80000000,0x80000000,0x80000000,
0x8000,0x8000,0x8000,0x8000,0x80000000,0x80008000,0x80008000,0x80008000,0x0,0x0,0x80000000,0x80000000,
0x80008000,0x8000,0x0,0x0,0x80000000,0x80008000,0x80008000,0x8000,0x0,0x0,0x80000000,0x80008000,
0x80008000,0x8000,0x0,0x80000000,0x80000000,0x80000000,0x80008000,0x0,0x8000,0x0,0x80000000,0x80008000,
0x80000000,0x8000,0x0,0x80000000,0x0,0x80000000,0x8000,0x0,0x80008000,0x0,0x80000000,0x8000,
0x80000000,0x8000,0x0,0x80008000,0x0,0x80000000,0x8000,0x80000000,0x80008000,0x0,0x80008000,0x0,
0x80000000,0x80008000,0x80000000,0x80008000,0x8000,0x80000000,0x8000,0x80000000,0x80000000,0x0,0x80008000,0x0,
0x0,0x80008000,0x80000000,0x8000,0x80008000,0x80000000,0x0,0x80008000,0x80000000,0x0,0x80008000,0x80000000,
0x0,0x80008000,0x80000000,0x0,0x80008000,0x80000000,0x0,0x8000,0x80000000,0x0,0x80008000,0x80000000,
0x8000,0x80008000,0x80000000,0x0,0x80000000,0x80000000,0x0,0x8000,0x0,0x8000,0x8000,0x80008000,
0x80008000,0x80008000,0x80000000,0x80000000,0x80000000,0x80000000,0x0,0x0,0x0,0x8000,0x8000,0x8000,
0x80008000,0x80008000,0x80008000,0x80008000,0x0,0x0,0x0,0x0,0x0,0x0,0x0,0x0,
0x0,0x0,0x0,0x0,0x0,0x0,0x0,0x0,0x0,0x0,0x0,0x0,
0x0,0x0,0x0,0x0,0x0,0x0,0x0,0x0,0x0,0x0,0x0,0x0,
0x0,0x0,0x0,0x0,0x0,0x0,0x0,0x0,0x0,0x0,0x0,0x0,
0x0,0x0,0x0,0x0,0x0,0x0,0x0,0x0,0x0,0x0,0x0,0x0,
0x0,0x0,0x0,0x0,0x0,0x0,0x0,0x0,0x0,0x0,0x0,0x0,
0x0,0x0,0x0,0x0,0x0,0x0,0x0,0x0,0x0,0x0,0x0,0x0,
0x0,0x0,0x0,0x0,0x0,0x0,0x0,0x0,0x0,0x0,0x0,0x0,
0x0,0x0,0x0,0x0,0x0,0x0,0x0,0x0,0x0,0x0,0x0,0x0,
0x0,0x0,0x0,0x0,0x0,0x0,0x0,0x0,0x0,0x0,0x0,0x0,
0x0,0x0,0x0,0x0,0x0,0x0,0x0,0x0,0x0,0x0,0x0,0x0,
0x0,0x0,0x0,0x0,0x0,0x0,0x0,0x0,0x0,0x0,0x0,0x0,
0x0,0x0,0x0,0x0,0x0,0x0,0x0,0x0,0x0,0x0,0x0,0x0,
0x0,0x0,0x0,0x0,0x0,0x0,0x0,0x0,0x0,0x0,0x0,0x0,
0x0,0x0,0x0,0x0,0x0,0x0,0x0,0x0,0x0,0x0,0x0,0x0,
0x0,0x0,0x0,0x0,0x0,0x0,0x0,0x0,0x0,0x0,0x0,0x0,
0x0,0x0,0x0,0x0,0x0,0x0,0x0,0x0,0x0,0x0,0x0,0x0,
0x0,0x0,0x0,0x0,0x0,0x0,0x0,0x0,0x0,0x0,0x0,0x0,
0x0,0x0,0x0,0x0,0x0,0x0,0x0,0x0,0x0,0x0,0x0,0x0,
0x0,0x0,0x0,0x0,0x0,0x0,0x0,0x0,0x0,0x0,0x0,0x0,
0x0,0x0,0x0,0x0,0x0,0x0,0x0,0x0,0x0,0x0,0x0,0x0,
0x0,0x0,0x0,0x0,0x0,0x0,0x0,0x0,
};
typedef GAS unsigned long long gu64;
#define RLX_AGENT __ATOMIC_RELAXED, __HIP_MEMORY_SCOPE_AGENT
#define SGB(mask, n) __builtin_amdgcn_sched_group_barrier(mask, n, 0)
#define M_MFMA 0x8
#define M_DSR 0x100
#define WAIT_LB0() __builtin_amdgcn_s_waitcnt(0xC07F)
#define WAITV(n) asm volatile("s_waitcnt vmcnt(%0)" :: "n"(n) : "memory")
#define PIN(x) asm volatile("" : "+v"(x))
#define HBAR do { __builtin_amdgcn_sched_barrier(0); __builtin_amdgcn_s_barrier(); __builtin_amdgcn_sched_barrier(0); } while (0)
DI bf16x8 mk_frag(unsigned a, unsigned b, unsigned c, unsigned d) { u32x4 v = {a, b, c, d}; return __builtin_bit_cast(bf16x8, v); }
DI bf16x8 as_frag(u32x4 v) { return __builtin_bit_cast(bf16x8, v); }
DI f32x16 mfma32(bf16x8 a, bf16x8 b, f32x16 c) { return __builtin_amdgcn_mfma_f32_32x32x16_bf16(a, b, c, 0, 0, 0); }
DI f32x4 mfma16(bf16x8 a, bf16x8 b, f32x4 c) { return __builtin_amdgcn_mfma_f32_16x16x32_bf16(a, b, c, 0, 0, 0); }
DI void glds16_sc1(const void* g, char LAS* l) { __builtin_amdgcn_global_load_lds((const unsigned GAS*)g, (unsigned LAS*)l, 16, 0, 16); }
DI void glds4(const void* g, char LAS* l) { __builtin_amdgcn_global_load_lds((const unsigned GAS*)g, (unsigned LAS*)l, 4, 0, 0); }
template <int W, int AUX> DI void dma(const void* sbase, unsigned voff, const char LAS* l) {
  const unsigned la = (unsigned)(size_t)l;
  if constexpr (W == 16 && AUX == 0) asm volatile("s_mov_b32 m0, %2\n\ts_nop 4\n\tglobal_load_lds_dwordx4 %0, %1" :: "v"(voff), "s"(sbase), "s"(la) : "memory");
  else if constexpr (W == 16) asm volatile("s_mov_b32 m0, %2\n\ts_nop 4\n\tglobal_load_lds_dwordx4 %0, %1 sc1" :: "v"(voff), "s"(sbase), "s"(la) : "memory");
  else asm volatile("s_mov_b32 m0, %2\n\ts_nop 4\n\tglobal_load_lds_dword %0, %1" :: "v"(voff), "s"(sbase), "s"(la) : "memory");
}
DI int opq(int v) { asm volatile("" : "+v"(v)); return v; }
DI float rbf(float f) { return bf_lo(cvtpk(f, 0.f)); }
typedef __amdgpu_buffer_rsrc_t rsrc_t;
DI rsrc_t mk_rsrc(const void* p, unsigned bytes) { return __builtin_amdgcn_make_buffer_rsrc((void*)p, 0, bytes, 0x00020000); }
#define ST32(v, rs, off) __builtin_amdgcn_raw_buffer_store_b32(v, rs, off, 0, 16)
#define ST64(v, rs, off) __builtin_amdgcn_raw_buffer_store_b64(v, rs, off, 0, 16)
#define ST128(v, rs, off) __builtin_amdgcn_raw_buffer_store_b128(v, rs, off, 0, 16)
#define LD32(rs, off) __builtin_amdgcn_raw_buffer_load_b32(rs, off, 0, 16)
#define LD64(rs, off) __builtin_amdgcn_raw_buffer_load_b64(rs, off, 0, 16)
#define LD128(rs, off) __builtin_amdgcn_raw_buffer_load_b128(rs, off, 0, 16)

constexpr size_t WS_EPOCH = 0, WS_FT = 4096, WS_FH = WS_FT + 2048, WS_FU = WS_FH + 2048, WS_FP = WS_FU + 2048, WS_FN = WS_FP + 2048;
constexpr size_t WS_TN = 16384, WS_UN = WS_TN + 8192;
constexpr size_t WS_T = 1u << 20, WS_U = WS_T + (4u << 20), WS_H = WS_U + (4u << 20), WS_P = WS_H + (15u << 20);
constexpr size_t WS_ZB = WS_P + (8u << 20);
constexpr size_t WS_STAMP = 200u << 20;
constexpr int G1_XB = 16384, G1_WB = 28672, G1_STAGE = G1_XB + G1_WB, G1_NT = 16;
constexpr int G2_HB = 16384, G2_STAGE = 32768, G2_NT = 28;
constexpr int LDS_TOTAL = 163840, LDS_SMALL = LDS_TOTAL - 512;
constexpr int LDS_DUMMY = LDS_SMALL + 256, LDS_PF = LDS_SMALL - 1024;
constexpr int P1_ZP = 2064, P1_ZA = 3 * G1_STAGE, P1_ZB = 3 * G2_STAGE;
constexpr int P1_GW = P1_ZA + 8 * P1_ZP, P1_TAB = P1_GW + 2048;
constexpr int E_ODD = 0;
constexpr int P2_TAB = P1_ZA;
constexpr int P3_TAB = P1_GW, P3_G = P3_TAB + 8192;
constexpr int P3_YP = 2112, P3_Y = 0, P3_V = 8 * P3_YP;
static_assert(P1_TAB + 8192 <= LDS_PF && P3_G + 2048 <= LDS_PF, "LDS map");

DI void touch(const void* sbase, unsigned voff, char LAS* lds) { dma<4, 0>(sbase, voff, lds + LDS_DUMMY); }
DI void pf1k(const void* sbase, int lane, char LAS* lds) { dma<16, 0>(sbase, (unsigned)(lane * 16), lds + LDS_PF); }
struct Args { const u16 *x, *wr, *wi, *w1, *b1, *w2, *b2, *gw; u16* out; char* ws; };

DI void pf_share(const Args& A, int cu, int j, int lane, char LAS* lds) {
  const char* p = j < 28 ? (const char*)A.w2 + (size_t)cu * 28672 + j * 1024 : j < 36 ? (const char*)A.wr + (size_t)cu * 8192 + (j - 28) * 1024 : (const char*)A.wi + (size_t)cu * 8192 + (j - 36) * 1024;
  pf1k(p, lane, lds);
}
#define STAMP(i) do {} while (0)

DI void poll_flags(const char* ws, size_t off, int n, unsigned long long tag, int lane) {
  gu64* p = (gu64*)(ws + off) + (lane < n ? lane : 0);
  int spins = 0;
  while (true) {
    unsigned long long v = __hip_atomic_load(p, RLX_AGENT);
    const bool done = __builtin_amdgcn_ballot_w64(v != tag) == 0ull || ++spins > (1 << 20);
    if (done) break;
    __builtin_amdgcn_s_sleep(1);
  }
  asm volatile("" ::: "memory");
}
DI void poll_flags256(const char* ws, size_t off, unsigned long long tag, int lane) {
  gu64* p = (gu64*)(ws + off) + lane;
  int spins = 0;
  while (true) {
    unsigned long long a = __hip_atomic_load(p, RLX_AGENT), b = __hip_atomic_load(p + 64, RLX_AGENT), c = __hip_atomic_load(p + 128, RLX_AGENT), d = __hip_atomic_load(p + 192, RLX_AGENT);
    const bool done = __builtin_amdgcn_ballot_w64(a != tag || b != tag || c != tag || d != tag) == 0ull || ++spins > (1 << 20);
    if (done) break;
    __builtin_amdgcn_s_sleep(1);
  }
  asm volatile("" ::: "memory");
}
DI void post_flag(char* ws, size_t off, unsigned long long tag) { __hip_atomic_store((gu64*)(ws + off), tag, RLX_AGENT); }

DI f32x2 gelu2(f32x2 v) {
  const float c1 = -2.0f * 0.7978845608028654f * 1.4426950408889634f;
  const float c2 = c1 * 0.044715f;
  f32x2 z = v * (v * v * c2 + c1);
  f32x2 d; d.x = __builtin_amdgcn_exp2f(z.x); d.y = __builtin_amdgcn_exp2f(z.y);
  d = d + 1.0f;
  f32x2 r; r.x = __builtin_amdgcn_rcpf(d.x); r.y = __builtin_amdgcn_rcpf(d.y);
  return v * r;
}
template <int N> DI float dpp_ror_add(float v) { return v + __builtin_bit_cast(float, __builtin_amdgcn_update_dpp(0, __builtin_bit_cast(int, v), 0x120 + N, 0xf, 0xf, false)); }
typedef __bf16 bf2_t __attribute__((ext_vector_type(2)));
DI float dot2(unsigned a, unsigned b, float c) { return __builtin_amdgcn_fdot2_f32_bf16(__builtin_bit_cast(bf2_t, a), __builtin_bit_cast(bf2_t, b), c, false); }

struct Tw { float r[16], i[16]; };
DI void cmul(float& r, float& i, float ar, float ai, float br, float bi) { r = ar * br - ai * bi; i = ar * bi + ai * br; }
template <int SG> DI void mk_tw(Tw& W, int u, int h) {
  float c1 = __builtin_amdgcn_cosf((float)u * (1.0f / 1024.0f)), s1 = SG * __builtin_amdgcn_sinf((float)u * (1.0f / 1024.0f));
  float c4 = __builtin_amdgcn_cosf((float)u * (4.0f / 1024.0f)), s4 = SG * __builtin_amdgcn_sinf((float)u * (4.0f / 1024.0f));
  float c16 = __builtin_amdgcn_cosf((float)u * (16.0f / 1024.0f)), s16 = SG * __builtin_amdgcn_sinf((float)u * (16.0f / 1024.0f));
  const float a0 = (float)(u * 2 * h) * (1.0f / 1024.0f);
  W.r[0] = __builtin_amdgcn_cosf(a0); W.i[0] = SG * __builtin_amdgcn_sinf(a0);
  cmul(W.r[2], W.i[2], W.r[0], W.i[0], c4, s4); cmul(W.r[4], W.i[4], W.r[2], W.i[2], c4, s4); cmul(W.r[6], W.i[6], W.r[4], W.i[4], c4, s4);
#pragma unroll
  for (int i = 0; i < 8; i += 2) cmul(W.r[i + 1], W.i[i + 1], W.r[i], W.i[i], c1, s1);
#pragma unroll
  for (int i = 0; i < 8; ++i) cmul(W.r[8 + i], W.i[8 + i], W.r[i], W.i[i], c16, s16);
}
typedef short s16x4v __attribute__((ext_vector_type(4)));
struct P1S { bf16x8 G; bf16x8 xf; s16x4 ga, gb; f32x4 gacc; int xa, za, gx, gwo; unsigned zo; };
DI void p1_setup(P1S& S, int s2, int q, int c4, int cu, int lw, int lane) { lane = opq(lane);
  const int col = lane & 15, g = lane >> 4;
  const int s1p = 8 * q + (col >> 1), c = col & 1;
  float gv[8];
#pragma unroll
  for (int j = 0; j < 8; ++j) { int idx = (s1p * (s2 + 64 * (8 * g + j))) & 2047; float r = (float)idx * (1.0f / 2048.0f) + (c ? 0.25f : 0.f);
    gv[j] = __builtin_amdgcn_cosf(r); }
  S.G = mk_frag(cvtpk(gv[0], gv[1]), cvtpk(gv[2], gv[3]), cvtpk(gv[4], gv[5]), cvtpk(gv[6], gv[7]));
  S.gacc = (f32x4){0.f, 0.f, 0.f, 0.f};
  const int qq = (lane & 15) >> 2, p = lane & 3, row = 32 * c4 + 8 * g + qq;
  S.xa = row * 128 + (((2 * lw + (p >> 1)) ^ (row & 7)) << 4) + 8 * (p & 1);
  S.za = P1_ZA + (col & 7) * P1_ZP + (16 * lw + 4 * g) * 2;
  const int grow = 32 * c4 + 16 * (q >> 1) + col;
  S.gx = grow * 128 + (((2 * lw + (g >> 1)) ^ (grow & 7)) << 4) + 8 * (g & 1);
  S.gwo = P1_GW + (16 * lw + 4 * g) * 2;
  S.zo = (col >> 3) ? (unsigned)((((cu * 4 + lw) * 16) * 64 + lane) * 8) : 0x7f000000u;
}
DI void p1_rd(P1S& S, const char LAS* lds, const char LAS* sb, int t) {
  S.xf = cat8(tr_read(sb + S.xa), tr_read(sb + ((S.xa ^ 64) + 512)));
  S.ga = *(const s16x4 LAS*)(sb + S.gx);
  S.gb = *(const s16x4 LAS*)(lds + S.gwo + t * 128);
}
DI void p1_col(P1S& S, char LAS* lds, rsrc_t rz, int t, int lane) {
  const f32x4 D = mfma16(S.xf, S.G, (f32x4){0.f, 0.f, 0.f, 0.f});
  S.gacc = __builtin_amdgcn_mfma_f32_16x16x16bf16_1k(S.ga, S.gb, S.gacc, 0, 0, 0);
  const u32x2 o = {cvtpk(D[0], D[1]), cvtpk(D[2], D[3])};
  __builtin_amdgcn_raw_buffer_store_b64(o, rz, S.zo + t * 512, 0, 0);
  if ((lane & 8) == 0) *(u32x2 LAS*)(lds + S.za + t * 128) = o;
}
struct P1B { u32x2 v[16]; };
DI void p1_fetch_b(P1B& B, const P1S& S, rsrc_t rz) {
#pragma unroll
  for (int t = 0; t < 16; ++t) B.v[t] = __builtin_amdgcn_raw_buffer_load_b64(rz, S.zo + t * 512, 0, 0);
}
DI void p1_finish_cols(const P1S& S, const P1B& B, char LAS* lds, int q, int lw, int lane) {
  if (lane & 8) {
#pragma unroll
    for (int t = 0; t < 16; ++t) *(u32x2 LAS*)(lds + S.za + (P1_ZB - P1_ZA) + t * 128) = B.v[t];
  }
  const int g = lane >> 4;
  if ((lane & 15) == 0 && (g >> 1) == (q & 1)) *(f32x4 LAS*)(lds + LDS_SMALL + (lw * 8 + 4 * (g & 1)) * 4) = S.gacc;
}
struct P1R { unsigned d[9]; };
DI P1R p1_row(char LAS* lds, int zbase, const Tw& W, int lane) { lane = opq(lane);
  const int h = lane >> 5, n1 = lane & 31;
  const char LAS* tab = lds + P1_TAB + lane * 16;
  bf16x8 Zf[4];
  { const int blk = (lane >> 4) & 1, qq = (lane & 15) >> 2, p = lane & 3;
    const char LAS* a = lds + zbase + (8 * h + qq) * 64 + (16 * blk + 4 * p) * 2;
#pragma unroll
    for (int ks = 0; ks < 4; ++ks) Zf[ks] = cat8(tr_read(a + (ks >> 1) * P1_ZP + (ks & 1) * 1024), tr_read(a + (ks >> 1) * P1_ZP + (ks & 1) * 1024 + 256)); }
  f32x16 V0 = (f32x16){}, V1 = (f32x16){};
#pragma unroll
  for (int ks = 0; ks < 4; ++ks) { u32x4 a = *(const u32x4 LAS*)(tab + ks * 1024);
    V0 = mfma32(as_frag(a), Zf[ks], V0); V1 = mfma32(as_frag(a ^ 0x80000000u), Zf[ks], V1); }
  float nr = (n1 & 1) ? -V0[0] : V0[0], ni = (n1 & 1) ? -V0[1] : V0[1];
  nr = dpp_ror_add<1>(nr); ni = dpp_ror_add<1>(ni); nr = dpp_ror_add<2>(nr); ni = dpp_ror_add<2>(ni);
  nr = dpp_ror_add<4>(nr); ni = dpp_ror_add<4>(ni); nr = dpp_ror_add<8>(nr); ni = dpp_ror_add<8>(ni);
  nr += __builtin_bit_cast(float, __builtin_amdgcn_readlane(__builtin_bit_cast(int, nr), 16)); ni += __builtin_bit_cast(float, __builtin_amdgcn_readlane(__builtin_bit_cast(int, ni), 16));
  char LAS* vb = lds + zbase;
  char LAS* vw = vb + h * 256 + (n1 & 3) * 4;
#pragma unroll
  for (int i = 0; i < 8; ++i) { const int sw = (((n1 >> 2) ^ h) ^ (2 * (i >> 1))) << 4, o = ((i & 1) + 4 * (i >> 1)) * 128;
    { const float wr = W.r[i], wi = W.i[i], vr = V0[2 * i], vi = V0[2 * i + 1]; *(unsigned LAS*)(vw + o + sw) = cvtpk(vr * wr - vi * wi, vr * wi + vi * wr); }
    { const float wr = W.r[8 + i], wi = W.i[8 + i], vr = V1[2 * i], vi = V1[2 * i + 1]; *(unsigned LAS*)(vw + 2048 + o + sw) = cvtpk(vr * wr - vi * wi, vr * wi + vi * wr); } }
  f32x16 D = (f32x16){};
  const int b = lane & 31;
#pragma unroll
  for (int ks = 0; ks < 4; ++ks) { bf16x8 Bf = *(const bf16x8 LAS*)(vb + b * 128 + (((2 * ks + h) ^ ((b >> 1) & 7)) << 4));
    D = mfma32(*(const bf16x8 LAS*)(tab + (4 + ks) * 1024), Bf, D); }
  P1R R;
#pragma unroll
  for (int i = 0; i < 8; ++i) R.d[i] = cvtpk(D[2 * i], D[2 * i + 1]);
  R.d[8] = cvtpk(nr, ni);
  return R;
}
DI void p1_store(const P1R& R, char* ws, int s2, int s1p, int lane) { lane = opq(lane);
  const int h = lane >> 5, b = lane & 31;
  rsrc_t rt = mk_rsrc(ws + WS_T, 4u << 20);
  const unsigned to = (unsigned)(((s1p * 64 + s2) * 512 + b + 64 * h) * 4);
#pragma unroll
  for (int i = 0; i < 8; ++i) ST32(R.d[i], rt, to + (32 * ((i & 1) + 4 * (i >> 1))) * 4);
  ST32(R.d[8], mk_rsrc(ws + WS_TN, 8192), lane == 0 ? (unsigned)((s2 * 32 + s1p) * 4) : 0x10000u);
}

struct G1L { const char* xg; const char* wg; unsigned xoff; unsigned woff[14]; };
DI void g1_lsetup(G1L& L, const u16* x, const u16* w1, int mi, int nj, int d, int lane) { lane = opq(lane);
  L.xg = (const char*)x + (size_t)(4 * mi + 2 * d) * 2048; L.wg = (const char*)w1 + nj * 448;
  L.xoff = (unsigned)((lane >> 3) * 64 * 2048 + (((lane & 7) ^ (lane >> 3)) * 16));
#pragma unroll
  for (int i = 0; i < 14; ++i) { int ci = (d * 14 + i) * 64 + lane; int k = ci / 28, c = ci % 28; c ^= ((k >> 3) & 1) << 1; L.woff[i] = (unsigned)(k * 7168 + c * 16); }
}
DI void g1_stage(const G1L& L, char LAS* lds, int t, int buf, int d) {
  const unsigned lx = (unsigned)(size_t)(lds + buf * G1_STAGE + d * 8192), lwb = (unsigned)(size_t)(lds + buf * G1_STAGE + G1_XB + d * 14336);
  const char* xs = L.xg + t * 128; const char* ws = L.wg + (size_t)t * 64 * 7168;
  asm volatile("s_mov_b32 m0, %[l]\n\ts_nop 4\n\tglobal_load_lds_dwordx4 %[xo], %[x0]\n\ts_add_u32 m0, m0, 0x400\n\ts_nop 0\n\tglobal_load_lds_dwordx4 %[xo], %[x1]\n\ts_add_u32 m0, m0, 0x400\n\ts_nop 0\n\tglobal_load_lds_dwordx4 %[xo], %[x2]\n\ts_add_u32 m0, m0, 0x400\n\ts_nop 0\n\tglobal_load_lds_dwordx4 %[xo], %[x3]\n\ts_add_u32 m0, m0, 0x400\n\ts_nop 0\n\tglobal_load_lds_dwordx4 %[xo], %[x4]\n\ts_add_u32 m0, m0, 0x400\n\ts_nop 0\n\tglobal_load_lds_dwordx4 %[xo], %[x5]\n\ts_add_u32 m0, m0, 0x400\n\ts_nop 0\n\tglobal_load_lds_dwordx4 %[xo], %[x6]\n\ts_add_u32 m0, m0, 0x400\n\ts_nop 0\n\tglobal_load_lds_dwordx4 %[xo], %[x7]" :: [l] "s"(lx), [x0] "s"(xs), [x1] "s"(xs + 0x100000), [x2] "s"(xs + 0x200000), [x3] "s"(xs + 0x300000),
               [x4] "s"(xs + 2048), [x5] "s"(xs + 0x100000 + 2048), [x6] "s"(xs + 0x200000 + 2048), [x7] "s"(xs + 0x300000 + 2048), [xo] "v"(L.xoff) : "memory", "scc");
  asm volatile("s_mov_b32 m0, %[l]\n\ts_nop 4\n\tglobal_load_lds_dwordx4 %[w0], %[ws]\n\ts_add_u32 m0, m0, 0x400\n\ts_nop 0\n\tglobal_load_lds_dwordx4 %[w1], %[ws]\n\ts_add_u32 m0, m0, 0x400\n\ts_nop 0\n\tglobal_load_lds_dwordx4 %[w2], %[ws]\n\ts_add_u32 m0, m0, 0x400\n\ts_nop 0\n\tglobal_load_lds_dwordx4 %[w3], %[ws]\n\ts_add_u32 m0, m0, 0x400\n\ts_nop 0\n\tglobal_load_lds_dwordx4 %[w4], %[ws]\n\ts_add_u32 m0, m0, 0x400\n\ts_nop 0\n\tglobal_load_lds_dwordx4 %[w5], %[ws]\n\ts_add_u32 m0, m0, 0x400\n\ts_nop 0\n\tglobal_load_lds_dwordx4 %[w6], %[ws]" :: [l] "s"(lwb), [ws] "s"(ws), [w0] "v"(L.woff[0]), [w1] "v"(L.woff[1]), [w2] "v"(L.woff[2]), [w3] "v"(L.woff[3]), [w4] "v"(L.woff[4]), [w5] "v"(L.woff[5]), [w6] "v"(L.woff[6]) : "memory", "scc");
  asm volatile("s_mov_b32 m0, %[l]\n\ts_nop 4\n\tglobal_load_lds_dwordx4 %[w0], %[ws]\n\ts_add_u32 m0, m0, 0x400\n\ts_nop 0\n\tglobal_load_lds_dwordx4 %[w1], %[ws]\n\ts_add_u32 m0, m0, 0x400\n\ts_nop 0\n\tglobal_load_lds_dwordx4 %[w2], %[ws]\n\ts_add_u32 m0, m0, 0x400\n\ts_nop 0\n\tglobal_load_lds_dwordx4 %[w3], %[ws]\n\ts_add_u32 m0, m0, 0x400\n\ts_nop 0\n\tglobal_load_lds_dwordx4 %[w4], %[ws]\n\ts_add_u32 m0, m0, 0x400\n\ts_nop 0\n\tglobal_load_lds_dwordx4 %[w5], %[ws]\n\ts_add_u32 m0, m0, 0x400\n\ts_nop 0\n\tglobal_load_lds_dwordx4 %[w6], %[ws]" :: [l] "s"(lwb + 7168), [ws] "s"(ws), [w0] "v"(L.woff[7]), [w1] "v"(L.woff[8]), [w2] "v"(L.woff[9]), [w3] "v"(L.woff[10]), [w4] "v"(L.woff[11]), [w5] "v"(L.woff[12]), [w6] "v"(L.woff[13]) : "memory", "scc");
}
struct G2L { const char* hg; const char* wg; unsigned hoff, woff, woff2; };
DI void g2_lsetup(G2L& L, const char* ht, const u16* w2, int mi, int ni, int kh, int lw, int lane) { lane = opq(lane);
  L.hg = ht + ((size_t)(8 * mi + 2 * lw) * 112 + 56 * kh) * 1024; L.hoff = lane * 16;
  L.wg = (const char*)w2 + (size_t)(1792 * kh) * 2048 + ni * 256;
  { int row = lw * 16 + (lane >> 4); int c = (lane & 15) ^ ((row & 7) << 1); L.woff = (unsigned)(row * 2048 + c * 16); }
  { int row = lw * 16 + 4 + (lane >> 4); int c = (lane & 15) ^ ((row & 7) << 1); L.woff2 = (unsigned)(row * 2048 + c * 16); }
}
#define HLD " sc1"
DI void g2_stageH(const G2L& L, char LAS* lds, int t, int buf, int lw) {
  const unsigned l = (unsigned)(size_t)(lds + buf * G2_STAGE + lw * 4096); const char* hs = L.hg + t * 2048;
  asm volatile("s_mov_b32 m0, %[l]\n\ts_nop 4\n\tglobal_load_lds_dwordx4 %[o], %[a0]" HLD "\n\ts_add_u32 m0, m0, 0x400\n\ts_nop 0\n\tglobal_load_lds_dwordx4 %[o], %[a1]" HLD "\n\ts_add_u32 m0, m0, 0x400\n\ts_nop 0\n\tglobal_load_lds_dwordx4 %[o], %[a2]" HLD "\n\ts_add_u32 m0, m0, 0x400\n\ts_nop 0\n\tglobal_load_lds_dwordx4 %[o], %[a3]" HLD "" :: [l] "s"(l), [a0] "s"(hs), [a1] "s"(hs + 1024), [a2] "s"(hs + 112 * 1024), [a3] "s"(hs + 113 * 1024), [o] "v"(L.hoff) : "memory", "scc");
}
DI void g2_stageW(const G2L& L, char LAS* lds, int t, int buf, int lw) {
  const unsigned l = (unsigned)(size_t)(lds + buf * G2_STAGE + G2_HB + lw * 4096); const char* ws = L.wg + (size_t)t * 64 * 2048;
  asm volatile("s_mov_b32 m0, %[l]\n\ts_nop 4\n\tglobal_load_lds_dwordx4 %[o], %[a0]\n\ts_add_u32 m0, m0, 0x400\n\ts_nop 0\n\tglobal_load_lds_dwordx4 %[p], %[a0]\n\ts_add_u32 m0, m0, 0x400\n\ts_nop 0\n\tglobal_load_lds_dwordx4 %[o], %[a1]\n\ts_add_u32 m0, m0, 0x400\n\ts_nop 0\n\tglobal_load_lds_dwordx4 %[p], %[a1]" :: [l] "s"(l), [a0] "s"(ws), [a1] "s"(ws + 8 * 2048), [o] "v"(L.woff), [p] "v"(L.woff2) : "memory", "scc");
}

struct P2D { unsigned t[16]; unsigned wr[16], wi[16]; unsigned y[16]; };
struct P2A { u32x4 f[16]; };
DI void p2_issue_t(P2D& I, const char* base, unsigned voff, unsigned tstride) {
#pragma unroll
  for (int kp = 0; kp < 4; ++kp) {
    const char* b = base + 16 * kp * tstride;
    asm volatile("s_nop 4\n\tglobal_load_dword %0, %4, %5 sc1\n\tglobal_load_dword %1, %4, %6 sc1\n\tglobal_load_dword %2, %4, %7 sc1\n\tglobal_load_dword %3, %4, %8 sc1"
                 : "=&v"(I.t[4 * kp]), "=&v"(I.t[4 * kp + 1]), "=&v"(I.t[4 * kp + 2]), "=&v"(I.t[4 * kp + 3])
                 : "v"(voff), "s"(b), "s"(b + tstride), "s"(b + 8 * tstride), "s"(b + 9 * tstride) : "memory");
  }
}
template <int G> DI void p2_issue_w(P2D& I, const u16* wr_, const u16* wi_, unsigned voff) {
  asm volatile("" : "+s"(wr_), "+s"(wi_));
#pragma unroll
  for (int mt = 2 * G; mt < 2 * G + 2; ++mt) {
    const char* a = (const char*)wr_ + (size_t)(8 * mt) * 32832; const char* b = (const char*)wi_ + (size_t)(8 * mt) * 32832;
    asm volatile("s_nop 4\n\tglobal_load_ushort %0, %4, %5\n\tglobal_load_ushort %1, %4, %6\n\tglobal_load_ushort %2, %4, %7\n\tglobal_load_ushort %3, %4, %8"
                 : "=&v"(I.wr[2 * mt]), "=&v"(I.wr[2 * mt + 1]), "=&v"(I.wi[2 * mt]), "=&v"(I.wi[2 * mt + 1])
                 : "v"(voff), "s"(a), "s"(a + 32832), "s"(b), "s"(b + 32832) : "memory");
  }
}
DI void p2_pin(P2D& I) {
#pragma unroll
  for (int i = 0; i < 16; ++i) { PIN(I.t[i]); PIN(I.wr[i]); PIN(I.wi[i]); }
}
DI u32x4 tab_neg(u32x4 v) { v[1] ^= 0x80008000u; v[3] ^= 0x80008000u; return v; }
DI void p2_tabs(P2A& A, const char LAS* tb) {
#pragma unroll
  for (int i = 0; i < 16; ++i) A.f[i] = *(const u32x4 LAS*)(tb + i * 1024);
}
template <int M4> DI void p2_fwd(P2D& I, const P2A& A) {
  f32x4 D0 = {0.f, 0.f, 0.f, 0.f}, D1 = {0.f, 0.f, 0.f, 0.f};
#pragma unroll
  for (int kp = 0; kp < 4; ++kp) { bf16x8 B = mk_frag(I.t[4 * kp], I.t[4 * kp + 1], I.t[4 * kp + 2], I.t[4 * kp + 3]);
    D0 = mfma16(as_frag(A.f[M4 * 4 + kp]), B, D0); D1 = mfma16(as_frag(tab_neg(A.f[M4 * 4 + kp])), B, D1); }
#pragma unroll
  for (int hf = 0; hf < 2; ++hf) { const int mt = M4 + 4 * hf; const f32x4 D = hf ? D1 : D0;
#pragma unroll
    for (int i = 0; i < 2; ++i) { const float a = __uint_as_float(I.wr[2 * mt + i] << 16), b = __uint_as_float(I.wi[2 * mt + i] << 16), xr = D[2 * i], xi = D[2 * i + 1];
      I.y[2 * mt + i] = cvtpk(xr * a - xi * b, -(xr * b) - xi * a); } }
}
template <int M4> DI void p2_inv(const P2D& I, const P2A& A, rsrc_t ru, unsigned uoff, unsigned ustride) {
  f32x4 D0 = {0.f, 0.f, 0.f, 0.f}, D1 = {0.f, 0.f, 0.f, 0.f};
#pragma unroll
  for (int kp = 0; kp < 4; ++kp) { bf16x8 B = mk_frag(I.y[4 * kp], I.y[4 * kp + 1], I.y[4 * kp + 2], I.y[4 * kp + 3]);
    D0 = mfma16(as_frag(A.f[M4 * 4 + kp]), B, D0); D1 = mfma16(as_frag(tab_neg(A.f[M4 * 4 + kp])), B, D1); }
#pragma unroll
  for (int hf = 0; hf < 2; ++hf) { const int mt = M4 + 4 * hf; const f32x4 D = hf ? D1 : D0;
#pragma unroll
    for (int i = 0; i < 2; ++i) ST32(cvtpk(D[2 * i], -D[2 * i + 1]), ru, uoff + (8 * mt + i) * ustride); }
}
DI unsigned long long flag_issue(const char* p, unsigned voff) { unsigned long long v; asm volatile("s_nop 4\n\tglobal_load_dwordx2 %0, %1, %2 sc1" : "=&v"(v) : "v"(voff), "s"(p) : "memory"); return v; }

DI void p3_ginv(char LAS* lds, int s2, int q, int t) { t = opq(t);
  const int row = t & 15, s1p = t >> 4;
  const int s1 = 8 * q + (row >> 1), cp = row & 1;
  const int idx = (s1p * (s2 + 64 * s1)) & 2047; const float r = (float)idx * (1.0f / 2048.0f);
  const float sc = 1.0f / (2048.0f * 1024.0f);
  const float gr = __builtin_amdgcn_cosf(r) * sc, gi = __builtin_amdgcn_sinf(r) * sc;
  *(unsigned LAS*)(lds + P3_G + (s1p >> 3) * 512 + (row + 16 * ((s1p >> 2) & 1)) * 16 + (s1p & 3) * 4) = cp ? cvtpk(gi, gr) : cvtpk(gr, -gi);
}
struct P3U { unsigned v[2][4][4]; unsigned nv[4][4]; };
DI void p3_issue(P3U& U, char* ws, int s2, int wid, int lane) { lane = opq(lane);
  const int cl = lane & 31, h = lane >> 5;
  rsrc_t ru = mk_rsrc(ws + WS_U, 4u << 20);
  const unsigned o = (unsigned)(((4 * h * 64 + s2) * 512 + 64 * wid + cl) * 4);
#pragma unroll
  for (int i = 0; i < 2; ++i)
#pragma unroll
    for (int ks = 0; ks < 4; ++ks)
#pragma unroll
      for (int m = 0; m < 4; ++m) U.v[i][ks][m] = LD32(ru, o + ((8 * ks + m) * 64 * 512 + 32 * i) * 4);
}
DI void p3_issue_nyq(P3U& U, char* ws, int s2, int lane) { lane = opq(lane);
  const int h = lane >> 5;
  rsrc_t rn = mk_rsrc(ws + WS_UN, 8192);
#pragma unroll
  for (int ks = 0; ks < 4; ++ks)
#pragma unroll
    for (int m = 0; m < 4; ++m) U.nv[ks][m] = LD32(rn, (s2 * 32 + 8 * ks + 4 * h + m) * 4);
}
DI void p3_c1(char LAS* lds, const P3U& U, int wid, int lane) { lane = opq(lane);
  const int cl = lane & 31, h = lane >> 5;
  bf16x8 G[4];
#pragma unroll
  for (int ks = 0; ks < 4; ++ks) G[ks] = *(const bf16x8 LAS*)(lds + P3_G + ks * 512 + ((lane & 15) + 16 * h) * 16);
  f32x16 D0 = (f32x16){}, D1 = (f32x16){};
#pragma unroll
  for (int ks = 0; ks < 4; ++ks) { D0 = mfma32(G[ks], mk_frag(U.v[0][ks][0], U.v[0][ks][1], U.v[0][ks][2], U.v[0][ks][3]), D0);
    D1 = mfma32(G[ks], mk_frag(U.v[1][ks][0], U.v[1][ks][1], U.v[1][ks][2], U.v[1][ks][3]), D1); }
  char LAS* yw = lds + P3_Y + 2 * h * P3_YP + (64 * wid + cl) * 4;
#pragma unroll
  for (int m = 0; m < 4; ++m) { const int o = ((m & 1) + 4 * (m >> 1)) * P3_YP;
    *(unsigned LAS*)(yw + o) = cvtpk(D0[2 * m], D0[2 * m + 1]); *(unsigned LAS*)(yw + o + 128) = cvtpk(D1[2 * m], D1[2 * m + 1]); }
}
DI void p3_c1_nyq(char LAS* lds, const P3U& U, int lane) { lane = opq(lane);
  const int cl = lane & 31, h = lane >> 5;
  f32x16 D = (f32x16){};
#pragma unroll
  for (int ks = 0; ks < 4; ++ks) D = mfma32(*(const bf16x8 LAS*)(lds + P3_G + ks * 512 + ((lane & 15) + 16 * h) * 16), mk_frag(U.nv[ks][0], U.nv[ks][1], U.nv[ks][2], U.nv[ks][3]), D);
  if (cl == 0) {
#pragma unroll
    for (int m = 0; m < 4; ++m) { const int s1l = (m & 1) + 4 * (m >> 1) + 2 * h; *(unsigned LAS*)(lds + P3_Y + s1l * P3_YP + 512 * 4) = cvtpk(D[2 * m], D[2 * m + 1]); } }
}
DI void p3_rowa(char LAS* lds, const Tw& W, int wid, int lane) { lane = opq(lane);
  const int h = lane >> 5, b = lane & 31, bc = b > 16 ? 16 : b;
  const char LAS* tab = lds + P3_TAB + lane * 16;
  const char LAS* yrow = lds + P3_Y + wid * P3_YP;
  bf16x8 Bf[4];
  { const char LAS* ya = yrow + (128 * h + bc) * 4; const char LAS* yb = yrow + (1024 - 128 * h - bc) * 4;
#pragma unroll
    for (int ks = 0; ks < 4; ++ks) { unsigned d[4];
#pragma unroll
      for (int m = 0; m < 4; ++m) d[m] = (ks < 2) ? *(const unsigned LAS*)(ya + (8 * ks + m) * 128) : *(const unsigned LAS*)(yb - (8 * ks + m) * 128);
      Bf[ks] = mk_frag(d[0], d[1], d[2], d[3]); } }
  f32x16 V0 = (f32x16){}, V1 = (f32x16){};
#pragma unroll
  for (int ks = 0; ks < 4; ++ks) { u32x4 a = *(const u32x4 LAS*)(tab + ks * 1024);
    u32x4 an = a; an[1] ^= 0x80008000u; an[3] ^= 0x80008000u;
    V0 = mfma32(as_frag(a), Bf[ks], V0); V1 = mfma32(as_frag(an), Bf[ks], V1); }
  char LAS* vb = lds + P3_V + wid * 4096;
  char LAS* vw = vb + h * 256 + (b & 3) * 4;
#pragma unroll
  for (int i = 0; i < 8; ++i) { const int sw = (((b >> 2) ^ h) ^ (2 * (i >> 1))) << 4, o = ((i & 1) + 4 * (i >> 1)) * 128;
    { const float wr = W.r[i], wi = W.i[i], vr = V0[2 * i], vi = V0[2 * i + 1]; *(unsigned LAS*)(vw + o + sw) = cvtpk(vr * wr - vi * wi, vr * wi + vi * wr); }
    { const float wr = W.r[8 + i], wi = W.i[8 + i], vr = V1[2 * i], vi = V1[2 * i + 1]; *(unsigned LAS*)(vw + 2048 + o + sw) = cvtpk(vr * wr - vi * wi, vr * wi + vi * wr); } }
}
DI f32x16 p3_rowb(char LAS* lds, int wid, int lane) { lane = opq(lane);
  const int h = lane >> 5;
  const char LAS* tab = lds + P3_TAB + lane * 16;
  const char LAS* vb = lds + P3_V + wid * 4096;
  const int rho = lane & 31; const int n1 = 16 * (rho >> 4) + 8 * ((rho >> 2) & 1) + 4 * ((rho >> 3) & 1) + (rho & 3);
  f32x16 D = (f32x16){};
#pragma unroll
  for (int ks = 0; ks < 3; ++ks) { bf16x8 Af = *(const bf16x8 LAS*)(vb + n1 * 128 + (((2 * ks + h) ^ ((n1 >> 1) & 7)) << 4));
    D = mfma32(Af, *(const bf16x8 LAS*)(tab + (4 + ks) * 1024), D); }
  return D;
}
#define PF_SLEEP 16
#define PF_W1 1
#define PF_T0 11
#define PFMODE 0
#define HELPER_PRIO 2
#define CODE_KB 32
#define LOADER_PRIO 2
__global__ __launch_bounds__(512, 2) void fused_kernel(Args A) {
  extern __shared__ __attribute__((aligned(16))) char smem[];
  char LAS* lds = (char LAS*)smem;
  const int bid = blockIdx.x, xc = bid & 7, idx = bid >> 3, cu = xc * 32 + idx;
  const int tid = threadIdx.x, wid = __builtin_amdgcn_readfirstlane(tid >> 6);
  char* ws = A.ws;
  const int mi1 = 8 * (xc & 1) + (idx & 7), nj = 4 * (xc >> 1) + (idx >> 3);
  const int c4 = nj >> 2, s2 = 4 * mi1 + c4, q = nj & 3;
  const int kh = xc & 1, mi2 = 8 * ((xc >> 1) & 1) + (idx & 7), ni = 4 * (xc >> 2) + (idx >> 3);
  unsigned long long LAS* tagp = (unsigned long long LAS*)(lds + LDS_SMALL + 128);
  const char* pc0; asm volatile("s_getpc_b64 %0" : "=s"(pc0));
  STAMP(0);
  if (wid >= 4) {
    const int lw = wid & 3, lane = tid & 63;
    __builtin_amdgcn_s_setprio(2);
    Tw W;
    P1R RA, RB;
    if (lw < 2) {
      const int d = lw;
      G1L L1; g1_lsetup(L1, A.x, A.w1, mi1, nj, d, lane);
      g1_stage(L1, lds, 0, 0, d); g1_stage(L1, lds, 1, 1, d);
      WAITV(22); BAR;
      STAMP(1);
      g1_stage(L1, lds, 2, 2, d);
      int buf = 0;
#pragma unroll 1
      for (int t = 0; t < G1_NT - 1; ++t) {
        if (t + 2 < G1_NT) { WAITV(22); } else { WAITV(0); }
        BAR;
        if (t + 3 < G1_NT) g1_stage(L1, lds, t + 3, buf, d);
        buf = buf + 1 == 3 ? 0 : buf + 1;
      }
      BAR;
      SCHED0; mk_tw<-1>(W, lane & 31, lane >> 5);
      STAMP(3);
    } else {
      const int hw = lw - 2;
      __builtin_amdgcn_s_setprio(HELPER_PRIO);
      dma<16, 0>((const char*)A.gw + hw * 1024, (unsigned)(lane * 16), lds + P1_GW + hw * 1024);
#pragma unroll
      for (int i = 0; i < 4; ++i) dma<16, 0>((const char*)TAB_P1 + (hw * 4 + i) * 1024, (unsigned)(lane * 16), lds + P1_TAB + (hw * 4 + i) * 1024);
      if (hw == 1) {
        const int ln = idx * (CODE_KB / 2) + (lane < CODE_KB / 2 ? lane : 0);
        dma<4, 0>(pc0, (unsigned)(ln * 64), lds + LDS_DUMMY);
      }
      P1S S0, S1; p1_setup(S0, s2, q, c4, cu, 2 * hw, lane); p1_setup(S1, s2, q, c4, cu, 2 * hw + 1, lane);
      rsrc_t rz = mk_rsrc(ws + WS_ZB, 8u << 20);
      WAITV(0); BAR;
      p1_rd(S0, lds, lds, 0); p1_rd(S1, lds, lds, 0);
      int buf = 0;
#pragma unroll 1
      for (int t = 0; t < G1_NT - 1; ++t) {
        buf = buf + 1 == 3 ? 0 : buf + 1;
        { const int per = (44 + 2 * (14 - PF_T0) - 1) / (2 * (14 - PF_T0));
          if (t >= PF_T0 && t <= 13) {
#pragma unroll 1
            for (int i = 0; i < per; ++i) { const int j = ((t - PF_T0) * 2 + hw) * per + i; if (j < 44) pf_share(A, cu, j, lane, lds); } } }
        WAIT_L(0); HBAR;
        p1_col(S0, lds, rz, t, lane); p1_col(S1, lds, rz, t, lane);
        p1_rd(S0, lds, lds + buf * G1_STAGE, t + 1); p1_rd(S1, lds, lds + buf * G1_STAGE, t + 1);
      }
      p1_col(S0, lds, rz, G1_NT - 1, lane); p1_col(S1, lds, rz, G1_NT - 1, lane);
      mk_tw<-1>(W, lane & 31, lane >> 5);
      WAITV(0);
      P1B B0, B1; p1_fetch_b(B0, S0, rz); p1_fetch_b(B1, S1, rz);
      WAIT_L(0); BAR;
      p1_finish_cols(S0, B0, lds, q, 2 * hw, lane); p1_finish_cols(S1, B1, lds, q, 2 * hw + 1, lane);
    }
    __builtin_amdgcn_s_setprio(2);
    G2L L2; g2_lsetup(L2, ws + WS_H, A.w2, mi2, ni, kh, lw, lane);
    g2_stageW(L2, lds, 0, 0, lw); g2_stageW(L2, lds, 1, 1, lw); g2_stageW(L2, lds, 2, 2, lw);
    RA = p1_row(lds, P1_ZA + 2 * lw * P1_ZP, W, lane);
    WAIT_L(0); BAR;
#pragma unroll
    for (int i = 0; i < 4; ++i) dma<16, 0>((const char*)TAB_P2 + (lw * 4 + i) * 1024, (unsigned)(lane * 16), lds + P2_TAB + (lw * 4 + i) * 1024);
    RB = p1_row(lds, P1_ZB + 2 * lw * P1_ZP, W, lane);
    p1_store(RA, ws, s2, 8 * q + lw, lane); p1_store(RB, ws, s2, 8 * q + 4 + lw, lane);
    STAMP(4);
    WAIT_L(0); BAR;
    const unsigned long long tag = *tagp;
    if (wid == 4 && lane == 0) post_flag(ws, WS_FH + (mi1 * 16 + nj) * 8, tag);
    STAMP(5);
    g2_stageW(L2, lds, 3, 3, lw);
#pragma unroll
    for (int i = 0; i < 2; ++i) dma<16, 0>((const char*)TAB_P3 + (lw * 2 + i) * 1024, (unsigned)(lane * 16), lds + P3_TAB + (lw * 2 + i) * 1024);
    poll_flags(ws, WS_FH + (mi2 * 16 + 8 * kh) * 8, 8, tag, lane);
    WAITV(0);
    STAMP(6);
    g2_stageH(L2, lds, 0, 0, lw); g2_stageH(L2, lds, 1, 1, lw); g2_stageH(L2, lds, 2, 2, lw); g2_stageH(L2, lds, 3, 3, lw);
    p3_ginv(lds, s2, q, lw * 64 + lane); p3_ginv(lds, s2, q, 256 + lw * 64 + lane);
    WAIT_L(0); WAITV(12); BAR;
    if (wid == 5 && lane == 0) post_flag(ws, WS_FT + (q * 64 + s2) * 8, tag);
    STAMP(7);
    const int s1p = cu >> 3, g16 = lane >> 4, kcol = 16 * (4 * (cu & 7) + lw) + (lane & 15);
    const bool nyq = (cu & 7) == 7 && lw == 3;
    const char LAS* tb = lds + P2_TAB + lane * 16;
    P2D P;
#define SST(t)
#define G2STAG(t, N, WORK) WAITV(N); BAR; SST(t); _Pragma("unroll 1") for (int ps_ = 0; ps_ < 2; ++ps_) { if (ps_ == (lw & 1)) { if (t + 4 < G2_NT) { g2_stageH(L2, lds, t + 4, (t) & 3, lw); g2_stageW(L2, lds, t + 4, (t) & 3, lw); } } else { WORK; } }
#define G2WORK(t, N, WORK) G2STEP(t, N); WORK
#define G2STEP(t, N) WAITV(N); BAR; SST(t); if (t + 4 < G2_NT) { g2_stageH(L2, lds, t + 4, (t) & 3, lw); g2_stageW(L2, lds, t + 4, (t) & 3, lw); }
    const unsigned wo = (unsigned)(((s1p + 64 * g16) * 513 + kcol) * 2), wn = (unsigned)(((s1p + 64 * g16) * 513 + 512) * 2);
    G2STEP(0, 8);  p2_issue_w<0>(P, A.wr, A.wi, wo);
    G2STEP(1, 20); p2_issue_w<1>(P, A.wr, A.wi, wo);
    G2STEP(2, 32); p2_issue_w<2>(P, A.wr, A.wi, wo);
    const unsigned long long fv = flag_issue(ws + WS_FT + (s1p >> 3) * 512, (unsigned)(lane * 8));
    G2STEP(3, 41); p2_issue_w<3>(P, A.wr, A.wi, wo);
    G2STEP(4, 41);
    WAITV(24);
    { unsigned long long f2 = fv; PIN(f2); if (__builtin_amdgcn_ballot_w64(f2 != tag) != 0ull) poll_flags(ws, WS_FT + (s1p >> 3) * 512, 64, tag, lane); }
    p2_issue_t(P, ws + WS_T + (size_t)s1p * 64 * 2048, (unsigned)((2 * g16 * 512 + kcol) * 4), 2048);
    P2A TA; p2_tabs(TA, tb);
    G2STEP(5, 49);
    G2STEP(6, 40);
    WAITV(16);
    p2_pin(P);
    p2_fwd<0>(P, TA);
    rsrc_t ru = mk_rsrc(ws + WS_U, 4u << 20); const unsigned uo = (unsigned)(((s1p * 64 + 2 * g16) * 512 + kcol) * 4);
    G2WORK(7, 16, p2_fwd<1>(P, TA));
    G2WORK(8, 16, p2_fwd<2>(P, TA));
    G2WORK(9, 16, p2_fwd<3>(P, TA));
    G2WORK(10, 16, p2_inv<0>(P, TA, ru, uo, 2048));
    G2WORK(11, 20, p2_inv<1>(P, TA, ru, uo, 2048));
    G2WORK(12, 24, p2_inv<2>(P, TA, ru, uo, 2048));
    G2WORK(13, 24, p2_inv<3>(P, TA, ru, uo, 2048));
#define G2STEPN(t, N, NN) if (nyq) { WAITV(NN); } else { WAITV(N); } BAR; SST(t); if (t + 4 < G2_NT) { g2_stageH(L2, lds, t + 4, (t) & 3, lw); g2_stageW(L2, lds, t + 4, (t) & 3, lw); }
    G2STEP(14, 24);
    if (nyq) { p2_issue_w<0>(P, A.wr, A.wi, wn); p2_issue_w<1>(P, A.wr, A.wi, wn); p2_issue_w<2>(P, A.wr, A.wi, wn); p2_issue_w<3>(P, A.wr, A.wi, wn); }
    G2STEPN(15, 20, 52);
    if (nyq) p2_issue_t(P, ws + WS_TN + s1p * 4, (unsigned)(2 * g16 * 128), 128);
    G2STEPN(16, 16, 63);
    G2STEPN(17, 16, 63);
    if (wid == 5 && lane == 0) post_flag(ws, WS_FU + cu * 8, tag);
    if (nyq) { WAITV(16); p2_pin(P); p2_fwd<0>(P, TA); }
    G2STEP(18, 16); if (nyq) p2_fwd<1>(P, TA);
    G2STEP(19, 16); if (nyq) { p2_fwd<2>(P, TA); p2_fwd<3>(P, TA); }
    rsrc_t rn = mk_rsrc(ws + WS_UN, 8192); const unsigned no = (lane & 15) == 0 ? (unsigned)(2 * g16 * 128 + s1p * 4) : 0x10000u;
    G2STEP(20, 16); if (nyq) p2_inv<0>(P, TA, rn, no, 128);
    G2STEPN(21, 16, 20); if (nyq) p2_inv<1>(P, TA, rn, no, 128);
    G2STEPN(22, 16, 24); if (nyq) p2_inv<2>(P, TA, rn, no, 128);
    G2STEPN(23, 16, 28); if (nyq) p2_inv<3>(P, TA, rn, no, 128);
    G2STEPN(24, 16, 28);
    G2STEPN(25, 8, 16);
    WAITV(0); if (nyq) post_flag(ws, WS_FN + s1p * 8, tag); poll_flags256(ws, WS_FU, tag, lane); BAR;
    STAMP(9);
    P3U U0, U1; p3_issue(U0, ws, s2, 2 * lw, lane); p3_issue(U1, ws, s2, 2 * lw + 1, lane);
    BAR;
    p3_c1(lds, U0, 2 * lw, lane);
    if (lw == 3) { poll_flags(ws, WS_FN, 32, tag, lane); p3_issue_nyq(U0, ws, s2, lane); }
    p3_c1(lds, U1, 2 * lw + 1, lane);
    if (lw == 3) p3_c1_nyq(lds, U0, lane);
  } else {
    const int wr = wid >> 1, wc = wid & 1;
    if (tid == 0) *tagp = __hip_atomic_load((gu64*)(ws + WS_EPOCH), RLX_AGENT) + 1ull;
    {
      const int lane = opq(tid & 63), fr = lane & 15, fq = lane >> 4;
      const int xra0 = (wr * 64 + fr) * 128 + ((fq ^ (fr & 7)) * 16), xra1 = xra0 ^ 64;
      const int g = fq, qq = (lane & 15) >> 2, p = lane & 3;
      const int wbase = G1_XB + (8 * g + qq) * 448 + (p >> 1) * 16 + (p & 1) * 8 + wc * 7 * 32;
      const int wA = wbase + (wc ? -32 : 32) * (g & 1), wB = wbase + (wc ? 32 : -32) * (g & 1);
      u32x2 bb[7];
      { const char* b1p = (const char*)A.b1 + (nj * 224 + wc * 112) * 2; const unsigned bo = (unsigned)(fq * 8);
        asm volatile("s_nop 4\n\tglobal_load_dwordx2 %0, %7, %8\n\tglobal_load_dwordx2 %1, %7, %8 offset:32\n\tglobal_load_dwordx2 %2, %7, %8 offset:64\n\tglobal_load_dwordx2 %3, %7, %8 offset:96\n\t"
                     "global_load_dwordx2 %4, %7, %8 offset:128\n\tglobal_load_dwordx2 %5, %7, %8 offset:160\n\tglobal_load_dwordx2 %6, %7, %8 offset:192"
                     : "=&v"(bb[0]), "=&v"(bb[1]), "=&v"(bb[2]), "=&v"(bb[3]), "=&v"(bb[4]), "=&v"(bb[5]), "=&v"(bb[6]) : "v"(bo), "s"(b1p) : "memory"); }
      f32x4 acc[7][4];
      bf16x8 X0[4], X1[4], W0[7], W1[7];
#define RDX(X, sb, a) _Pragma("unroll") for (int rt = 0; rt < 4; ++rt) X[rt] = *(const bf16x8 LAS*)((sb) + (a) + rt * 2048);
#define RDW(W, sb, kh_) _Pragma("unroll") for (int ft = 0; ft < 7; ++ft) { const char LAS* a0 = (sb) + ((ft & 1) ? wB : wA) + ft * 32 + (kh_) * 32 * 448; W[ft] = cat8(tr_read(a0), tr_read(a0 + 4 * 448)); }
#define MMA(W, X) _Pragma("unroll") for (int ft = 0; ft < 7; ++ft) _Pragma("unroll") for (int rt = 0; rt < 4; ++rt) acc[ft][rt] = mfma16(W[ft], X[rt], acc[ft][rt]);
#define ILV() _Pragma("unroll") for (int i_ = 0; i_ < 18; ++i_) { SGB(M_MFMA, 1); SGB(M_DSR, 1); } SGB(M_MFMA, 10);
      __builtin_amdgcn_s_sleep(PF_SLEEP);
#pragma unroll
      for (int i = 0; i < 4; ++i) pf1k((const char*)A.x + (size_t)cu * 16384 + (wid * 4 + i) * 1024, lane, lds);
#pragma unroll
      for (int i = 0; i < 7; ++i) pf1k((const char*)A.w1 + (size_t)cu * 28672 + (wid * 7 + i) * 1024, lane, lds);
      WAITV(4 + 7 * PF_W1 + (PFMODE == 1 ? 11 : 0));
#pragma unroll
      for (int ft = 0; ft < 7; ++ft) { PIN(bb[ft]);
        const f32x4 bv = {bf_lo(bb[ft].x), bf_hi(bb[ft].x), bf_lo(bb[ft].y), bf_hi(bb[ft].y)};
#pragma unroll
        for (int rt = 0; rt < 4; ++rt) acc[ft][rt] = bv; }
      WAIT_LB0();
      __builtin_amdgcn_s_setprio(1);
      BAR;
      STAMP(1);
      RDX(X0, lds, xra0); RDW(W0, lds, 0);
      SCHED0;
      int buf = 0;
#pragma unroll 1
      for (int t = 0; t < G1_NT - 1; ++t) {
        const char LAS* sb = lds + buf * G1_STAGE;
        buf = buf + 1 == 3 ? 0 : buf + 1;
        const char LAS* sn = lds + buf * G1_STAGE;
        RDX(X1, sb, xra1); RDW(W1, sb, 1);
        MMA(W0, X0);
        ILV();
        SCHED0;
        WAIT_LB0(); BAR;
        SCHED0;
        RDX(X0, sn, xra0); RDW(W0, sn, 0);
        MMA(W1, X1);
        ILV();
        SCHED0;
      }
      { const char LAS* sb = lds + buf * G1_STAGE;
        RDX(X1, sb, xra1); RDW(W1, sb, 1);
        MMA(W0, X0);
        ILV();
        SCHED0;
        WAIT_LB0(); BAR;
        SCHED0;
        MMA(W1, X1); }
      __builtin_amdgcn_s_setprio(0);
      STAMP(3);
      SCHED0;
      rsrc_t rh = mk_rsrc(ws + WS_H, 64 * 224 * 1024);
#define HAUX 16
#define STH(v, rs, off) __builtin_amdgcn_raw_buffer_store_b128(v, rs, off, 0, HAUX)
      const unsigned ho = (unsigned)(((mi1 * 8 + wr * 4) * 112 + nj * 7) * 1024 + lane * 16);
#define GELU_PK(ft) { f32x4 a_ = acc[ft][rt]; f32x2 lo_ = gelu2((f32x2){a_[0], a_[1]}), hi_ = gelu2((f32x2){a_[2], a_[3]}); pk[ft][0] = cvtpk(lo_.x, lo_.y); pk[ft][1] = cvtpk(hi_.x, hi_.y); }
      unsigned odd[4][2];
#pragma unroll
      for (int rt = 0; rt < 4; ++rt) { const f32x4 a_ = wc ? acc[0][rt] : acc[6][rt]; f32x2 lo_ = gelu2((f32x2){a_[0], a_[1]}), hi_ = gelu2((f32x2){a_[2], a_[3]});
        odd[rt][0] = cvtpk(lo_.x, lo_.y); odd[rt][1] = cvtpk(hi_.x, hi_.y);
        if (wc) *(u32x2 LAS*)(lds + E_ODD + ((wr * 4 + rt) * 64 + lane) * 8) = (u32x2){odd[rt][0], odd[rt][1]}; }
#pragma unroll
      for (int rt = 0; rt < 4; ++rt) {
        unsigned pk[7][2];
        const unsigned hr = ho + rt * 112 * 1024;
        if (wc == 0) {
#pragma unroll
          for (int j = 0; j < 3; ++j) { GELU_PK(2 * j); GELU_PK(2 * j + 1); STH(((u32x4){pk[2 * j][0], pk[2 * j][1], pk[2 * j + 1][0], pk[2 * j + 1][1]}), rh, hr + j * 1024); SCHED0; }
        } else {
#pragma unroll
          for (int j = 0; j < 3; ++j) { GELU_PK(2 * j + 1); GELU_PK(2 * j + 2); STH(((u32x4){pk[2 * j + 1][0], pk[2 * j + 1][1], pk[2 * j + 2][0], pk[2 * j + 2][1]}), rh, hr + (4 + j) * 1024); SCHED0; }
        }
        if (rt == 1) { WAIT_LB0(); BAR; SCHED0;
          if (wc == 0) {
#pragma unroll
            for (int r2 = 0; r2 < 4; ++r2) { const u32x2 o2 = *(const u32x2 LAS*)(lds + E_ODD + ((wr * 4 + r2) * 64 + lane) * 8);
              STH(((u32x4){odd[r2][0], odd[r2][1], o2.x, o2.y}), rh, ho + r2 * 112 * 1024 + 3 * 1024); }
            SCHED0; } }
      }
      STAMP(4);
      WAITV(0); BAR;
      STAMP(5);
      BAR;
      STAMP(7);
    }
    {
      const int lane = opq(tid & 63), fr = lane & 15, fq = lane >> 4;
      const int g = lane >> 4, qq = (lane & 15) >> 2, p = lane & 3;
      const int ha = wr * 4 * 2048 + lane * 16;
      int wa[4];
#pragma unroll
      for (int nt = 0; nt < 4; ++nt) wa[nt] = G2_HB + (4 * g + qq) * 256 + 32 * ((4 * wc + nt) ^ (4 * (g & 1) + qq)) + 16 * (p >> 1) + 8 * (p & 1);
      f32x4 acc[4][4];
#pragma unroll
      for (int i = 0; i < 4; ++i)
#pragma unroll
        for (int j = 0; j < 4; ++j) acc[i][j] = (f32x4){0.f, 0.f, 0.f, 0.f};
      bf16x8 H0[4], H1[4], W0[4], W1[4];
#define G2RD(H, W, sb, kl) { _Pragma("unroll") for (int rt = 0; rt < 4; ++rt) H[rt] = *(const bf16x8 LAS*)((sb) + ha + rt * 2048 + (kl) * 1024); \
    _Pragma("unroll") for (int nt = 0; nt < 4; ++nt) { const char LAS* a0 = (sb) + wa[nt] + (kl) * 8192; W[nt] = cat8(tr_read(a0), tr_read(a0 + 4096)); } }
#define G2MMA(W, H) _Pragma("unroll") for (int nt = 0; nt < 4; ++nt) _Pragma("unroll") for (int rt = 0; rt < 4; ++rt) acc[nt][rt] = mfma16(W[nt], H[rt], acc[nt][rt]);
#define G2ILV() _Pragma("unroll") for (int i_ = 0; i_ < 6; ++i_) { SGB(M_MFMA, 1); SGB(M_DSR, 2); } SGB(M_MFMA, 10);
      __builtin_amdgcn_s_setprio(1);
      G2RD(H0, W0, lds, 0);
      SCHED0;
      int buf = 0;
#pragma unroll 1
      for (int t = 0; t < G2_NT - 1; ++t) {
        const char LAS* sb = lds + buf * G2_STAGE;
        buf = (buf + 1) & 3;
        const char LAS* sn = lds + buf * G2_STAGE;
        G2RD(H1, W1, sb, 1);
        G2MMA(W0, H0);
        G2ILV();
        SCHED0;
        WAIT_LB0(); BAR;
        SCHED0;
        G2RD(H0, W0, sn, 0);
        G2MMA(W1, H1);
        G2ILV();
        SCHED0;
      }
      { const char LAS* sb = lds + buf * G2_STAGE;
        G2RD(H1, W1, sb, 1);
        G2MMA(W0, H0);
        SCHED0;
        WAIT_LB0(); BAR;
        SCHED0;
        G2MMA(W1, H1); }
      __builtin_amdgcn_s_setprio(0);
      STAMP(9);
      rsrc_t rp = mk_rsrc(ws + WS_P, 8u << 20);
      const unsigned po = (unsigned)(((kh * 2048 + mi2 * 128 + wr * 64 + fr) * 1024 + ni * 128 + wc * 64 + 16 * (fq & 1) + 8 * (fq >> 1)) * 2);
#pragma unroll
      for (int rt = 0; rt < 4; ++rt)
#pragma unroll
        for (int np = 0; np < 2; ++np) {
          f32x4 Xv = acc[2 * np][rt], Yv = acc[2 * np + 1][rt];
          auto s0 = __builtin_amdgcn_permlane16_swap(cvtpk(Xv[0], Xv[1]), cvtpk(Yv[0], Yv[1]), false, false);
          auto s1 = __builtin_amdgcn_permlane16_swap(cvtpk(Xv[2], Xv[3]), cvtpk(Yv[2], Yv[3]), false, false);
          ST128(((u32x4){s0[0], s1[0], s0[1], s1[1]}), rp, po + (rt * 16 * 1024 + np * 32) * 2);
        }
    }
  }
  {
    const int lane = opq(tid & 63);
    STAMP(10);
    Tw W3; { const int b = lane & 31; mk_tw<1>(W3, b > 16 ? 16 : b, lane >> 5); }
    WAITV(0); WAIT_L(0); BAR;
    const unsigned long long tag = *tagp;
    if (wid == 4 && lane == 0) { post_flag(ws, WS_FP + ((mi2 * 8 + ni) * 2 + kh) * 8, tag); if (cu == 0) post_flag(ws, WS_EPOCH, tag); }
    STAMP(11);
    p3_rowa(lds, W3, wid, lane);
    const float LAS* gsum = (const float LAS*)(lds + LDS_SMALL);
    float lg = 0.f;
#pragma unroll
    for (int w = 0; w < 4; ++w) lg += gsum[w * 8 + wid];
    const float g = rbf(1.0f / (1.0f + __expf(-rbf(lg)))), og = rbf(1.0f - g);
    const int n2 = lane & 31, h = lane >> 5;
    STAMP(12);
    poll_flags(ws, WS_FP + mi1 * 128, 16, tag, lane);
    STAMP(13);
    rsrc_t rp = mk_rsrc(ws + WS_P, 8u << 20);
    const int prow = mi1 * 128 + 32 * c4 + 8 * q + wid, row = s2 + 64 * (8 * q + wid);
    u32x4 bb[2], p0[2], p1[2];
#pragma unroll
    for (int uu = 0; uu < 2; ++uu) { const int col = 32 * n2 + 16 * uu + 8 * h;
      p0[uu] = LD128(rp, (prow * 1024 + col) * 2); p1[uu] = LD128(rp, ((2048 + prow) * 1024 + col) * 2); bb[uu] = *(const u32x4*)(A.b2 + col); }
    SCHED0;
    const f32x16 D = p3_rowb(lds, wid, lane);
#pragma unroll
    for (int uu = 0; uu < 2; ++uu) {
      const int col = 32 * n2 + 16 * uu + 8 * h;
      u32x4 o;
#pragma unroll
      for (int m = 0; m < 4; ++m) {
        float v[2];
#pragma unroll
        for (int e = 0; e < 2; ++e) {
          float s = e ? bf_hi(p0[uu][m]) + bf_hi(p1[uu][m]) : bf_lo(p0[uu][m]) + bf_lo(p1[uu][m]);
          float bv = rbf(rbf(s) + (e ? bf_hi(bb[uu][m]) : bf_lo(bb[uu][m])));
          float av = rbf(D[8 * uu + 2 * m + e]);
          v[e] = rbf(g * av) + rbf(og * bv);
        }
        o[m] = cvtpk(v[0], v[1]);
      }
      *(u32x4*)(A.out + (size_t)row * 1024 + col) = o;
    }
    STAMP(14);
  }
}
extern "C" void kernel_launch(void* const* d_in, const int* in_sizes, int n_in, void* d_out, int out_size, void* d_ws, size_t ws_size, hipStream_t s) {
  Args A;
  A.x = (const u16*)d_in[0]; A.wr = (const u16*)d_in[1]; A.wi = (const u16*)d_in[2];
  A.w1 = (const u16*)d_in[4]; A.b1 = (const u16*)d_in[5]; A.w2 = (const u16*)d_in[6]; A.b2 = (const u16*)d_in[7]; A.gw = (const u16*)d_in[8];
  A.out = (u16*)d_out; A.ws = (char*)d_ws;
  (void)hipFuncSetAttribute((const void*)fused_kernel, hipFuncAttributeMaxDynamicSharedMemorySize, LDS_TOTAL);
  fused_kernel<<<256, 512, LDS_TOTAL, s>>>(A);
}
```
